```python
import torch

def _softmax(x, axis=-1):
    return torch.softmax(x, dim=axis)
def _gelu(x):
    return torch.nn.functional.gelu(x, approximate="tanh")
def _silu(x):
    return torch.nn.functional.silu(x)
def _sigmoid(x):
    return torch.sigmoid(x)
def _layernorm(x, axis=-1, eps=1e-5):
    m = x.mean(dim=axis, keepdim=True)
    v = x.var(dim=axis, keepdim=True, unbiased=False)
    return (x - m) / torch.sqrt(v + eps)
def _scatter_add(tgt, idx, src, axis=0):
    return tgt.clone().index_add_(axis, idx, src)
def _segment_sum(src, seg, n):
    out = torch.zeros((n,) + src.shape[1:], dtype=src.dtype)
    return out.index_add_(0, seg, src)
def _pad(x, pw):
    flat = tuple(v for pair in reversed(pw) for v in pair)
    return torch.nn.functional.pad(x, flat)

kernel_name = 'concat_heads__mha_block_cached__mha_block_cached__near_ridge'

def setup_inputs():

    _w = (lambda a: (lambda t: t.to(torch.bfloat16) if t.is_floating_point() else t)(torch.as_tensor(a)))
    def _A_setup():
        B, Sq, D = 8, 144, 640
        H, Sk, Dh = 32, 1024, 128
        return {
            "x": _w((lambda *s: torch.randn(*s, dtype=torch.bfloat16) * 0.8)(B, Sq, D)),
            "k_cache": _w((lambda *s: torch.randn(*s, dtype=torch.bfloat16) * 0.8)(B, H, Sk, Dh)),
            "v_cache": _w((lambda *s: torch.randn(*s, dtype=torch.bfloat16) * 0.05)(B, H, Sk, Dh)),
            "wq": _w((lambda *s: torch.randn(*s, dtype=torch.bfloat16) * 0.8)(D, H * Dh) * (D ** -0.5)),
            "wo": _w((lambda *s: torch.randn(*s, dtype=torch.bfloat16) * 0.05)(H * Dh, D) * ((H * Dh) ** -0.5)),
        }
    _A_in = _A_setup()

    def _B_setup():
        B, Sq, D = 8, 144, 640
        H, Sk, Dh = 32, 1024, 128
        return {
            "x": _w((lambda *s: torch.randn(*s, dtype=torch.bfloat16) * 0.8)(B, Sq, D)),
            "k_cache": _w((lambda *s: torch.randn(*s, dtype=torch.bfloat16) * 0.8)(B, H, Sk, Dh)),
            "v_cache": _w((lambda *s: torch.randn(*s, dtype=torch.bfloat16) * 0.05)(B, H, Sk, Dh)),
            "wq": _w((lambda *s: torch.randn(*s, dtype=torch.bfloat16) * 0.8)(D, H * Dh) * (D ** -0.5)),
            "wo": _w((lambda *s: torch.randn(*s, dtype=torch.bfloat16) * 0.05)(H * Dh, D) * ((H * Dh) ** -0.5)),
        }
    _B_in = _B_setup()

    return {
        "A_x": _A_in["x"],
        "A_k_cache": _A_in["k_cache"],
        "A_v_cache": _A_in["v_cache"],
        "A_wq": _A_in["wq"],
        "A_wo": _A_in["wo"],
        "B_x": _B_in["x"],
        "B_k_cache": _B_in["k_cache"],
        "B_v_cache": _B_in["v_cache"],
        "B_wq": _B_in["wq"],
        "B_wo": _B_in["wo"],
    }

def reference(A_x, A_k_cache, A_v_cache, A_wq, A_wo, B_x, B_k_cache, B_v_cache, B_wq, B_wo):
    def _A_ref(x, k_cache, v_cache, wq, wo):
        B, Sq, D = x.shape
        H, Dh = 32, 128
        q = (lambda x, *d: x.permute(*d))((x @ wq).reshape(B, Sq, H, Dh), 0, 2, 1, 3)
        scores = torch.einsum("bhqd,bhkd->bhqk", q, k_cache) * (Dh ** -0.5)
        p = _softmax(scores, axis=-1)
        o = torch.einsum("bhqk,bhkd->bhqd", p, v_cache)
        return (lambda x, *d: x.permute(*d))(o, 0, 2, 1, 3).reshape(B, Sq, H * Dh) @ wo

    def _B_ref(x, k_cache, v_cache, wq, wo):
        B, Sq, D = x.shape
        H, Dh = 32, 128
        q = (lambda x, *d: x.permute(*d))((x @ wq).reshape(B, Sq, H, Dh), 0, 2, 1, 3)
        scores = torch.einsum("bhqd,bhkd->bhqk", q, k_cache) * (Dh ** -0.5)
        p = _softmax(scores, axis=-1)
        o = torch.einsum("bhqk,bhkd->bhqd", p, v_cache)
        return (lambda x, *d: x.permute(*d))(o, 0, 2, 1, 3).reshape(B, Sq, H * Dh) @ wo

    a = _A_ref(A_x, A_k_cache, A_v_cache, A_wq, A_wo)
    b = _B_ref(B_x, B_k_cache, B_v_cache, B_wq, B_wo)
    return torch.concatenate([a, b], axis=-1)
```

```cpp
#include <hip/hip_runtime.h>
#include <hip/hip_bf16.h>
#include <stdint.h>

constexpr int NB_ = 8, SQ = 144, DM = 640, NH = 32, SK = 1024, DH = 128;
constexpr int HD = NH * DH;
constexpr int MROWS = NB_ * SQ;
constexpr float QSCALE = 0.088388347648318447f * 1.4426950408889634f;

using bf16x8 = __attribute__((ext_vector_type(8))) short;
using s16x4 = __attribute__((ext_vector_type(4))) short;
using f32x4 = __attribute__((ext_vector_type(4))) float;
using u32x4 = __attribute__((ext_vector_type(4))) unsigned;
using u32x2 = __attribute__((ext_vector_type(2))) unsigned;
typedef short v4i16_t __attribute__((ext_vector_type(4)));
typedef __attribute__((address_space(3))) const char* lds_cptr;
typedef unsigned short u16;

#define MFMA16(a, b, c) __builtin_amdgcn_mfma_f32_16x16x32_bf16(a, b, c, 0, 0, 0)

typedef __bf16 bf16x2_t __attribute__((ext_vector_type(2)));
typedef float f32x2_t __attribute__((ext_vector_type(2)));
__device__ __forceinline__ unsigned cvtpk(float lo, float hi) {
  const f32x2_t v = {lo, hi};
  return __builtin_bit_cast(unsigned, __builtin_convertvector(v, bf16x2_t));
}
__device__ __forceinline__ s16x4 vtr(lds_cptr p) {
  return __builtin_bit_cast(s16x4, __builtin_amdgcn_ds_read_tr16_b64_v4i16((__attribute__((address_space(3))) v4i16_t*)p));
}
__device__ __forceinline__ bf16x8 ldsb128(lds_cptr p) {
  return *(const __attribute__((address_space(3))) bf16x8*)p;
}
__device__ __forceinline__ void glds16(const void* sbase, unsigned voff, unsigned lds_base) {
  unsigned sv;
  asm volatile("s_mov_b32 %0, m0\n\ts_mov_b32 m0, %3\n\ts_nop 0\n\tglobal_load_lds_dwordx4 %1, %2\n\ts_mov_b32 m0, %0"
               : "=&s"(sv) : "v"(voff), "s"(sbase), "s"(lds_base) : "memory");
}

template <int NB>
__device__ __forceinline__ void gemm_simple(const u16* __restrict__ W, int ldw, const u16* __restrict__ X, int ldx,
                                            int nchunks, char* lds, f32x4 (&acc)[NB][8], int tid) {
  const int lane = tid & 63, g = lane >> 4, i16 = lane & 15;
  lds_cptr l3 = (lds_cptr)lds;
  for (int kc = 0; kc < nchunks; ++kc) {
    __syncthreads();
#pragma unroll
    for (int it = 0; it < 4; ++it) {
      const int p = it * 256 + tid, row = p >> 4, ch = p & 15;
      const u32x4 v = *(const u32x4*)(W + (size_t)(kc * 64 + row) * ldw + ch * 8);
      const int f = (row & 3) | ((row >> 1) & 4);
      *(u32x4*)(lds + row * 256 + ((ch ^ (f << 1)) << 4)) = v;
    }
    __syncthreads();
#pragma unroll
    for (int kk = 0; kk < 2; ++kk) {
      bf16x8 xb[NB];
#pragma unroll
      for (int qb = 0; qb < NB; ++qb)
        xb[qb] = *(const bf16x8*)(X + (size_t)(qb * 16 + i16) * ldx + kc * 64 + kk * 32 + g * 8);
#pragma unroll
      for (int db = 0; db < 8; ++db) {
        const int r0 = kk * 32 + 8 * g + (i16 >> 2);
        const int f0 = (r0 & 3) | ((r0 >> 1) & 4);
        const int a0 = r0 * 256 + ((db ^ f0) << 5) + (lane & 3) * 8;
        const s16x4 lo = vtr(l3 + a0);
        const s16x4 hi = vtr(l3 + a0 + 4 * 256);
        const bf16x8 wf = {lo[0], lo[1], lo[2], lo[3], hi[0], hi[1], hi[2], hi[3]};
#pragma unroll
        for (int qb = 0; qb < NB; ++qb) acc[qb][db] = MFMA16(wf, xb[qb], acc[qb][db]);
      }
    }
  }
}

template <int NB>
__device__ __forceinline__ void qproj_wave(const u16* x, const u16* wq, u16* qout, char* lds, int tid, int qb0) {
  f32x4 acc[NB][8];
#pragma unroll
  for (int a = 0; a < NB; ++a)
#pragma unroll
    for (int b = 0; b < 8; ++b) acc[a][b] = f32x4{0.f, 0.f, 0.f, 0.f};
  gemm_simple<NB>(wq, HD, x + (size_t)qb0 * 16 * DM, DM, DM / 64, lds, acc, tid);
  const int lane = tid & 63, g = lane >> 4, i16 = lane & 15;
#pragma unroll
  for (int qb = 0; qb < NB; ++qb)
#pragma unroll
    for (int db = 0; db < 8; ++db) {
      u32x2 v = {cvtpk(acc[qb][db][0] * QSCALE, acc[qb][db][1] * QSCALE), cvtpk(acc[qb][db][2] * QSCALE, acc[qb][db][3] * QSCALE)};
      *(u32x2*)(qout + (size_t)((qb0 + qb) * 16 + i16) * DH + db * 16 + g * 4) = v;
    }
}

__global__ __launch_bounds__(256, 2) void qproj_kernel(const u16* __restrict__ xA, const u16* __restrict__ xB,
                                                       const u16* __restrict__ wqA, const u16* __restrict__ wqB,
                                                       u16* __restrict__ qws) {
  __shared__ __attribute__((aligned(16))) char lds[16384];
  const int item = blockIdx.x, blk = item >> 8, b = (item >> 5) & 7, h = item & 31;
  const u16* x = (blk ? xB : xA) + (size_t)b * SQ * DM;
  const u16* wq = (blk ? wqB : wqA) + h * DH;
  u16* qo = qws + (size_t)item * SQ * DH;
  const int tid = threadIdx.x, wid = __builtin_amdgcn_readfirstlane(tid >> 6);
  if (wid == 0) qproj_wave<3>(x, wq, qo, lds, tid, 0);
  else qproj_wave<2>(x, wq, qo, lds, tid, 1 + 2 * wid);
}

template <int NB>
__device__ __forceinline__ void oproj_wave(const u16* o, const u16* wo, float* part, char* lds, int tid, int qb0) {
  f32x4 acc[NB][8];
#pragma unroll
  for (int a = 0; a < NB; ++a)
#pragma unroll
    for (int b = 0; b < 8; ++b) acc[a][b] = f32x4{0.f, 0.f, 0.f, 0.f};
  gemm_simple<NB>(wo, DM, o + (size_t)qb0 * 16 * HD, HD, 1024 / 64, lds, acc, tid);
  const int lane = tid & 63, g = lane >> 4, i16 = lane & 15;
#pragma unroll
  for (int qb = 0; qb < NB; ++qb)
#pragma unroll
    for (int db = 0; db < 8; ++db)
      *(f32x4*)(part + (size_t)((qb0 + qb) * 16 + i16) * DM + db * 16 + g * 4) = acc[qb][db];
}

__global__ __launch_bounds__(256, 2) void oproj_kernel(const u16* __restrict__ ows, const u16* __restrict__ woA,
                                                       const u16* __restrict__ woB, float* __restrict__ part) {
  __shared__ __attribute__((aligned(16))) char lds[16384];
  const int id = blockIdx.x, ksp = id & 3, nt = (id >> 2) % 5, bb = id / 20, blk = bb >> 3;
  const u16* o = ows + (size_t)bb * SQ * HD + ksp * 1024;
  const u16* wo = (blk ? woB : woA) + (size_t)ksp * 1024 * DM + nt * 128;
  float* p = part + ((size_t)ksp * 16 + bb) * SQ * DM + nt * 128;
  const int tid = threadIdx.x, wid = __builtin_amdgcn_readfirstlane(tid >> 6);
  if (wid == 0) oproj_wave<3>(o, wo, p, lds, tid, 0);
  else oproj_wave<2>(o, wo, p, lds, tid, 1 + 2 * wid);
}

__global__ __launch_bounds__(256) void oreduce_kernel(const float* __restrict__ part, u16* __restrict__ out) {
  const int idx = blockIdx.x * 256 + threadIdx.x;
  const int total = 16 * SQ * DM / 4;
  if (idx >= total) return;
  f32x4 s = *(const f32x4*)(part + (size_t)idx * 4);
#pragma unroll
  for (int k = 1; k < 4; ++k) s += *(const f32x4*)(part + (size_t)k * 16 * SQ * DM + (size_t)idx * 4);
  const int e = idx * 4, n = e % DM, row = (e / DM) % SQ, bb = e / (DM * SQ), blk = bb >> 3, b = bb & 7;
  u32x2 v = {cvtpk(s[0], s[1]), cvtpk(s[2], s[3])};
  *(u32x2*)(out + ((size_t)(b * SQ + row) * 2 + blk) * DM + n) = v;
}

constexpr int SLOT = 16384, NSLOT = 5, NTILE = SK / 32;
constexpr float THR = 6.0f;

template <int NB>
__device__ __forceinline__ void attn_wave(const u16* __restrict__ Qg, const u16* Kg, const u16* Vg, u16* __restrict__ Og,
                                          char* lds, int tid, int wid, int qb0) {
  const int lane = tid & 63, g = lane >> 4, i16 = lane & 15;
  lds_cptr l3 = (lds_cptr)lds;
  const unsigned lds0 = (unsigned)(uintptr_t)lds;
  const u16* src = (wid < 2) ? Kg : Vg;
  unsigned voff[4];
#pragma unroll
  for (int it = 0; it < 4; ++it) {
    const int row = (wid & 1) * 16 + it * 4 + g;
    const int ch = (wid < 2) ? (i16 ^ (row & 15)) : (i16 ^ ((row & 7) << 1));
    voff[it] = row * 256 + ch * 16;
  }
  const unsigned ldst = lds0 + wid * 4096;
#define ISSUE(t, slot) do { _Pragma("unroll") for (int it = 0; it < 4; ++it) \
    glds16(src, voff[it] + (unsigned)(t) * 8192u, (unsigned)__builtin_amdgcn_readfirstlane(ldst + (slot) * SLOT + it * 1024)); } while (0)
  ISSUE(0, 0); ISSUE(1, 1); ISSUE(2, 2); ISSUE(3, 3);

  bf16x8 qf[NB][4];
#pragma unroll
  for (int qb = 0; qb < NB; ++qb)
#pragma unroll
    for (int ks = 0; ks < 4; ++ks) qf[qb][ks] = *(const bf16x8*)(Qg + (size_t)((qb0 + qb) * 16 + i16) * DH + ks * 32 + g * 8);

  f32x4 o[NB][8], ls[NB], negm[NB];
#pragma unroll
  for (int qb = 0; qb < NB; ++qb) {
#pragma unroll
    for (int db = 0; db < 8; ++db) o[qb][db] = f32x4{0.f, 0.f, 0.f, 0.f};
    ls[qb] = f32x4{0.f, 0.f, 0.f, 0.f};
    negm[qb] = f32x4{0.f, 0.f, 0.f, 0.f};
  }
  const bf16x8 ones = {0x3F80, 0x3F80, 0x3F80, 0x3F80, 0x3F80, 0x3F80, 0x3F80, 0x3F80};
  const int kbase = i16 * 256 + ((g ^ i16) << 4);
  const int vrow = 4 * g + (i16 >> 2);
  const int vbase = 8192 + vrow * 256 + ((vrow & 7) << 5) + (lane & 3) * 8;

  int slot = 0;
  for (int t = 0; t < NTILE; ++t) {
    if (t + 3 < NTILE) asm volatile("s_waitcnt vmcnt(12) lgkmcnt(0)\n\ts_barrier" ::: "memory");
    else if (t + 2 < NTILE) asm volatile("s_waitcnt vmcnt(8) lgkmcnt(0)\n\ts_barrier" ::: "memory");
    else if (t + 1 < NTILE) asm volatile("s_waitcnt vmcnt(4) lgkmcnt(0)\n\ts_barrier" ::: "memory");
    else asm volatile("s_waitcnt vmcnt(0) lgkmcnt(0)\n\ts_barrier" ::: "memory");
    if (t + 4 < NTILE) { const int ns = (slot == 0) ? (NSLOT - 1) : (slot - 1); ISSUE(t + 4, ns); }
    const int so = slot * SLOT;
    f32x4 s[2][NB];
#pragma unroll
    for (int ks = 0; ks < 4; ++ks)
#pragma unroll
      for (int kb = 0; kb < 2; ++kb) {
        const bf16x8 kf = ldsb128(l3 + so + (kbase ^ (ks << 6)) + kb * 4096);
#pragma unroll
        for (int qb = 0; qb < NB; ++qb) s[kb][qb] = MFMA16(kf, qf[qb][ks], ks == 0 ? negm[qb] : s[kb][qb]);
      }
    bf16x8 pf[NB];
#pragma unroll
    for (int qb = 0; qb < NB; ++qb) {
      float mx = fmaxf(fmaxf(s[0][qb][0], s[0][qb][1]), fmaxf(s[0][qb][2], s[0][qb][3]));
      mx = fmaxf(mx, fmaxf(fmaxf(s[1][qb][0], s[1][qb][1]), fmaxf(s[1][qb][2], s[1][qb][3])));
      if (t == 0 || __any(mx > THR)) {
        float bm = mx;
        { auto r = __builtin_amdgcn_permlane16_swap(__float_as_uint(bm), __float_as_uint(bm), false, false);
          bm = fmaxf(__uint_as_float(r[0]), __uint_as_float(r[1])); }
        { auto r = __builtin_amdgcn_permlane32_swap(__float_as_uint(bm), __float_as_uint(bm), false, false);
          bm = fmaxf(__uint_as_float(r[0]), __uint_as_float(r[1])); }
        const float d = (t == 0) ? bm : fmaxf(bm, 0.f);
        negm[qb] -= d; s[0][qb] -= d; s[1][qb] -= d;
        if (t != 0) {
          const float al = __builtin_amdgcn_exp2f(-d);
#pragma unroll
          for (int db = 0; db < 8; ++db) o[qb][db] *= al;
          ls[qb] *= al;
        }
      }
      u32x4 w;
      w[0] = cvtpk(__builtin_amdgcn_exp2f(s[0][qb][0]), __builtin_amdgcn_exp2f(s[0][qb][1]));
      w[1] = cvtpk(__builtin_amdgcn_exp2f(s[0][qb][2]), __builtin_amdgcn_exp2f(s[0][qb][3]));
      w[2] = cvtpk(__builtin_amdgcn_exp2f(s[1][qb][0]), __builtin_amdgcn_exp2f(s[1][qb][1]));
      w[3] = cvtpk(__builtin_amdgcn_exp2f(s[1][qb][2]), __builtin_amdgcn_exp2f(s[1][qb][3]));
      pf[qb] = __builtin_bit_cast(bf16x8, w);
    }
#pragma unroll
    for (int db = 0; db < 8; ++db) {
      const s16x4 lo = vtr(l3 + so + (vbase ^ (db << 5)));
      const s16x4 hi = vtr(l3 + so + (vbase ^ (db << 5)) + 4096);
      const bf16x8 vf = {lo[0], lo[1], lo[2], lo[3], hi[0], hi[1], hi[2], hi[3]};
#pragma unroll
      for (int qb = 0; qb < NB; ++qb) o[qb][db] = MFMA16(vf, pf[qb], o[qb][db]);
    }
#pragma unroll
    for (int qb = 0; qb < NB; ++qb) ls[qb] = MFMA16(ones, pf[qb], ls[qb]);
    slot = (slot == NSLOT - 1) ? 0 : slot + 1;
  }
#undef ISSUE
#pragma unroll
  for (int qb = 0; qb < NB; ++qb) {
    const float inv = 1.0f / ls[qb][0];
#pragma unroll
    for (int db = 0; db < 8; ++db) {
      u32x2 v = {cvtpk(o[qb][db][0] * inv, o[qb][db][1] * inv), cvtpk(o[qb][db][2] * inv, o[qb][db][3] * inv)};
      *(u32x2*)(Og + (size_t)((qb0 + qb) * 16 + i16) * HD + db * 16 + g * 4) = v;
    }
  }
}

__global__ __launch_bounds__(256, 2) void attn_kernel(const u16* __restrict__ qws, const u16* __restrict__ kA, const u16* __restrict__ vA,
                                                      const u16* __restrict__ kB, const u16* __restrict__ vB, u16* __restrict__ ows) {
  extern __shared__ __attribute__((aligned(16))) char lds[];
  const int item = blockIdx.x, blk = item >> 8, bh = item & 255, b = bh >> 5, h = bh & 31;
  const u16* K = (blk ? kB : kA) + (size_t)bh * SK * DH;
  const u16* V = (blk ? vB : vA) + (size_t)bh * SK * DH;
  const u16* Q = qws + (size_t)item * SQ * DH;
  u16* O = ows + ((size_t)(blk * 8 + b) * SQ) * HD + h * DH;
  const int tid = threadIdx.x, wid = __builtin_amdgcn_readfirstlane(tid >> 6);
  if (wid == 0) attn_wave<3>(Q, K, V, O, lds, tid, wid, 0);
  else attn_wave<2>(Q, K, V, O, lds, tid, wid, 1 + 2 * wid);
}

extern "C" void kernel_launch(void* const* d_in, const int* in_sizes, int n_in, void* d_out, int out_size, void* d_ws,
                              size_t ws_size, hipStream_t stream) {
  const u16* xA = (const u16*)d_in[0]; const u16* kA = (const u16*)d_in[1]; const u16* vA = (const u16*)d_in[2];
  const u16* wqA = (const u16*)d_in[3]; const u16* woA = (const u16*)d_in[4];
  const u16* xB = (const u16*)d_in[5]; const u16* kB = (const u16*)d_in[6]; const u16* vB = (const u16*)d_in[7];
  const u16* wqB = (const u16*)d_in[8]; const u16* woB = (const u16*)d_in[9];
  char* ws = (char*)d_ws;
  u16* qws = (u16*)ws;
  u16* ows = (u16*)(ws + (size_t)512 * SQ * DH * 2);
  float* part = (float*)(ws + (size_t)2 * 512 * SQ * DH * 2);
  hipFuncSetAttribute((const void*)attn_kernel, hipFuncAttributeMaxDynamicSharedMemorySize, NSLOT * SLOT);
  qproj_kernel<<<512, 256, 0, stream>>>(xA, xB, wqA, wqB, qws);
  attn_kernel<<<512, 256, NSLOT * SLOT, stream>>>(qws, kA, vA, kB, vB, ows);
  oproj_kernel<<<320, 256, 0, stream>>>(ows, woA, woB, part);
  oreduce_kernel<<<(16 * SQ * DM / 4 + 255) / 256, 256, 0, stream>>>(part, (u16*)d_out);
}
```

```cpp
#include <hip/hip_runtime.h>
#include <hip/hip_bf16.h>
#include <stdint.h>

constexpr int NB_ = 8, SQ = 144, DM = 640, NH = 32, SK = 1024, DH = 128;
constexpr int HD = NH * DH;
constexpr int MROWS = NB_ * SQ;
constexpr float QSCALE = 0.088388347648318447f * 1.4426950408889634f;

using bf16x8 = __attribute__((ext_vector_type(8))) short;
using s16x4 = __attribute__((ext_vector_type(4))) short;
using f32x4 = __attribute__((ext_vector_type(4))) float;
using u32x4 = __attribute__((ext_vector_type(4))) unsigned;
using u32x2 = __attribute__((ext_vector_type(2))) unsigned;
typedef short v4i16_t __attribute__((ext_vector_type(4)));
typedef __attribute__((address_space(3))) const char* lds_cptr;
typedef unsigned short u16;

#define SBAR() __builtin_amdgcn_sched_barrier(0)
#define PIN(x) asm volatile("" : "+v"(x))
#define MFMA16(a, b, c) __builtin_amdgcn_mfma_f32_16x16x32_bf16(a, b, c, 0, 0, 0)

typedef __bf16 bf16x2_t __attribute__((ext_vector_type(2)));
typedef float f32x2_t __attribute__((ext_vector_type(2)));
__device__ __forceinline__ unsigned cvtpk(float lo, float hi) {
  const f32x2_t v = {lo, hi};
  return __builtin_bit_cast(unsigned, __builtin_convertvector(v, bf16x2_t));
}
__device__ __forceinline__ s16x4 vtr(lds_cptr p) {
  return __builtin_bit_cast(s16x4, __builtin_amdgcn_ds_read_tr16_b64_v4i16((__attribute__((address_space(3))) v4i16_t*)p));
}
__device__ __forceinline__ bf16x8 ldsb128(lds_cptr p) {
  return *(const __attribute__((address_space(3))) bf16x8*)p;
}
__device__ __forceinline__ float fmx(float a, float b) { return __builtin_elementwise_maximum(a, b); }
__device__ __forceinline__ void glds16c(const void* sbase, unsigned voff, unsigned lds_base) {
  unsigned sv;
  asm volatile("s_mov_b32 %0, m0\n\ts_mov_b32 m0, %3\n\ts_nop 0\n\tglobal_load_lds_dwordx4 %1, %2\n\ts_mov_b32 m0, %0"
               : "=&s"(sv) : "v"(voff), "s"(sbase), "s"(lds_base) : "memory");
}
__device__ __forceinline__ void glds16(const void* sbase, unsigned voff, unsigned lds_base) {
  unsigned sv;
  asm volatile("s_mov_b32 %0, m0\n\ts_mov_b32 m0, %3\n\ts_nop 0\n\tglobal_load_lds_dwordx4 %1, %2 nt\n\ts_mov_b32 m0, %0"
               : "=&s"(sv) : "v"(voff), "s"(sbase), "s"(lds_base) : "memory");
}

template <int NB>
__device__ __forceinline__ void gemm_simple(const u16* __restrict__ W, int ldw, const u16* __restrict__ X, int ldx,
                                            int nchunks, char* lds, f32x4 (&acc)[NB][8], int tid) {
  const int lane = tid & 63, g = lane >> 4, i16 = lane & 15;
  lds_cptr l3 = (lds_cptr)lds;
  for (int kc = 0; kc < nchunks; ++kc) {
    __syncthreads();
#pragma unroll
    for (int it = 0; it < 4; ++it) {
      const int p = it * 256 + tid, row = p >> 4, ch = p & 15;
      const u32x4 v = *(const u32x4*)(W + (size_t)(kc * 64 + row) * ldw + ch * 8);
      const int f = (row & 3) | ((row >> 1) & 4);
      *(u32x4*)(lds + row * 256 + ((ch ^ (f << 1)) << 4)) = v;
    }
    __syncthreads();
#pragma unroll
    for (int kk = 0; kk < 2; ++kk) {
      bf16x8 xb[NB];
#pragma unroll
      for (int qb = 0; qb < NB; ++qb)
        xb[qb] = *(const bf16x8*)(X + (size_t)(qb * 16 + i16) * ldx + kc * 64 + kk * 32 + g * 8);
#pragma unroll
      for (int db = 0; db < 8; ++db) {
        const int r0 = kk * 32 + 8 * g + (i16 >> 2);
        const int f0 = (r0 & 3) | ((r0 >> 1) & 4);
        const int a0 = r0 * 256 + ((db ^ f0) << 5) + (lane & 3) * 8;
        const s16x4 lo = vtr(l3 + a0);
        const s16x4 hi = vtr(l3 + a0 + 4 * 256);
        const bf16x8 wf = {lo[0], lo[1], lo[2], lo[3], hi[0], hi[1], hi[2], hi[3]};
#pragma unroll
        for (int qb = 0; qb < NB; ++qb) acc[qb][db] = MFMA16(wf, xb[qb], acc[qb][db]);
      }
    }
  }
}

template <int NB>
__device__ __forceinline__ void qproj_wave(const u16* x, const u16* wq, u16* qout, char* lds, int tid, int qb0) {
  f32x4 acc[NB][8];
#pragma unroll
  for (int a = 0; a < NB; ++a)
#pragma unroll
    for (int b = 0; b < 8; ++b) acc[a][b] = f32x4{0.f, 0.f, 0.f, 0.f};
  gemm_simple<NB>(wq, HD, x + (size_t)qb0 * 16 * DM, DM, DM / 64, lds, acc, tid);
  const int lane = tid & 63, g = lane >> 4, i16 = lane & 15;
#pragma unroll
  for (int qb = 0; qb < NB; ++qb)
#pragma unroll
    for (int db = 0; db < 8; ++db) {
      u32x2 v = {cvtpk(acc[qb][db][0] * QSCALE, acc[qb][db][1] * QSCALE), cvtpk(acc[qb][db][2] * QSCALE, acc[qb][db][3] * QSCALE)};
      *(u32x2*)(qout + (size_t)((qb0 + qb) * 16 + i16) * DH + db * 16 + g * 4) = v;
    }
}

__global__ __launch_bounds__(256, 2) void qproj_kernel(const u16* __restrict__ xA, const u16* __restrict__ xB,
                                                       const u16* __restrict__ wqA, const u16* __restrict__ wqB,
                                                       u16* __restrict__ qws) {
  __shared__ __attribute__((aligned(16))) char lds[16384];
  const int item = blockIdx.x, blk = item >> 8, b = (item >> 5) & 7, h = item & 31;
  const u16* x = (blk ? xB : xA) + (size_t)b * SQ * DM;
  const u16* wq = (blk ? wqB : wqA) + h * DH;
  u16* qo = qws + (size_t)item * SQ * DH;
  const int tid = threadIdx.x, wid = __builtin_amdgcn_readfirstlane(tid >> 6);
  if (wid == 0) qproj_wave<3>(x, wq, qo, lds, tid, 0);
  else qproj_wave<2>(x, wq, qo, lds, tid, 1 + 2 * wid);
}

#define OXL glds16c
#define OWL glds16c
constexpr int OP_STAGE = 18432 + 16384, OP_NSTG = 4, OP_SPLIT = 3;
template <int MB, int NDMA>
__device__ __forceinline__ void oproj_wave(const u16* X, const u16* W, float* __restrict__ part, char* lds, int tid, int wid, int nst) {
  const int lane = tid & 63, g = lane >> 4, i16 = lane & 15;
  lds_cptr l3 = (lds_cptr)lds;
  const unsigned lds0 = (unsigned)(uintptr_t)lds;
  const int ng = wid & 3, mb0 = (wid >> 2) * 5;
  unsigned xoff[3], woff[2];
#pragma unroll
  for (int it = 0; it < 3; ++it) { const int row = (wid + 8 * it) * 8 + (lane >> 3); xoff[it] = row * 128 + (((lane & 7) ^ (row & 7)) << 4); }
#pragma unroll
  for (int it = 0; it < 2; ++it) { const int row = (wid + 8 * it) * 4 + g; const int f = (row & 3) | ((row >> 1) & 4); woff[it] = row * (DM * 2) + ((i16 ^ (f << 1)) << 4); }
#define OISSUE(s_) do { const unsigned sb_ = lds0 + ((s_) & 3) * OP_STAGE; \
    OXL(X, xoff[0] + (unsigned)(s_) * 18432u, (unsigned)__builtin_amdgcn_readfirstlane(sb_ + wid * 1024)); \
    OXL(X, xoff[1] + (unsigned)(s_) * 18432u, (unsigned)__builtin_amdgcn_readfirstlane(sb_ + (wid + 8) * 1024)); \
    if (NDMA == 5) OXL(X, xoff[2] + (unsigned)(s_) * 18432u, (unsigned)__builtin_amdgcn_readfirstlane(sb_ + (wid + 16) * 1024)); \
    OWL(W, woff[0] + (unsigned)(s_) * (64u * DM * 2u), (unsigned)__builtin_amdgcn_readfirstlane(sb_ + 18432 + wid * 1024)); \
    OWL(W, woff[1] + (unsigned)(s_) * (64u * DM * 2u), (unsigned)__builtin_amdgcn_readfirstlane(sb_ + 18432 + (wid + 8) * 1024)); } while (0)
  f32x4 acc[2][MB];
#pragma unroll
  for (int a = 0; a < 2; ++a)
#pragma unroll
    for (int b = 0; b < MB; ++b) acc[a][b] = f32x4{0.f, 0.f, 0.f, 0.f};
  OISSUE(0); OISSUE(1); OISSUE(2);
  const int xrd = (mb0 * 16 + i16) * 128 + ((g ^ (i16 & 7)) << 4);
  const int wr = 8 * g + (i16 >> 2);
  const int wrd = 18432 + wr * 256 + ((((wr & 3) | ((wr >> 1) & 4))) << 5) + (lane & 3) * 8;
  for (int s = 0; s < nst; ++s) {
    if (s + 2 < nst) asm volatile("s_waitcnt vmcnt(%0) lgkmcnt(0)\n\ts_barrier" :: "i"(2 * NDMA) : "memory");
    else if (s + 1 < nst) asm volatile("s_waitcnt vmcnt(%0) lgkmcnt(0)\n\ts_barrier" :: "i"(NDMA) : "memory");
    else asm volatile("s_waitcnt vmcnt(0) lgkmcnt(0)\n\ts_barrier" ::: "memory");
    const int so = (s & 3) * OP_STAGE;
    bf16x8 wf[2][2], xf[2][MB];
#pragma unroll
    for (int kk = 0; kk < 2; ++kk) {
#pragma unroll
      for (int nb = 0; nb < 2; ++nb) {
        const s16x4 lo = vtr(l3 + so + (wrd ^ ((2 * ng + nb) << 5)) + kk * 8192);
        const s16x4 hi = vtr(l3 + so + (wrd ^ ((2 * ng + nb) << 5)) + kk * 8192 + 1024);
        wf[kk][nb] = (bf16x8){lo[0], lo[1], lo[2], lo[3], hi[0], hi[1], hi[2], hi[3]};
      }
#pragma unroll
      for (int mb = 0; mb < MB; ++mb) xf[kk][mb] = ldsb128(l3 + so + (xrd ^ (kk << 6)) + mb * 2048);
    }
    SBAR();
    if (s + 3 < nst) OISSUE(s + 3);
    SBAR();
#pragma unroll
    for (int kk = 0; kk < 2; ++kk)
#pragma unroll
      for (int mb = 0; mb < MB; ++mb)
#pragma unroll
        for (int nb = 0; nb < 2; ++nb) acc[nb][mb] = MFMA16(wf[kk][nb], xf[kk][mb], acc[nb][mb]);
    SBAR();
  }
#undef OISSUE
#pragma unroll
  for (int mb = 0; mb < MB; ++mb)
#pragma unroll
    for (int nb = 0; nb < 2; ++nb)
      *(f32x4*)(part + (size_t)((mb0 + mb) * 16 + i16) * DM + (2 * ng + nb) * 16 + g * 4) = acc[nb][mb];
}

__global__ __launch_bounds__(512, 2) void oproj_kernel(const u16* __restrict__ ows, const u16* __restrict__ woA,
                                                       const u16* __restrict__ woB, float* __restrict__ part) {
  extern __shared__ __attribute__((aligned(16))) char lds[];
  const int L = (blockIdx.x & 7) * 30 + (blockIdx.x >> 3), grp = L / 40, r = L % 40, blk = grp / 3, ksp = grp % 3, nt = r % 5, bb = blk * 8 + r / 5;
  const int s0 = ksp == 0 ? 0 : (ksp == 1 ? 22 : 43), nst = ksp == 0 ? 22 : 21;
  const u16* o = ows + (size_t)bb * SQ * HD + (size_t)s0 * SQ * 64;
  const u16* wo = (blk ? woB : woA) + (size_t)s0 * 64 * DM + nt * 128;
  float* p = part + ((size_t)ksp * 16 + bb) * SQ * DM + nt * 128;
  const int tid = threadIdx.x, wid = __builtin_amdgcn_readfirstlane(tid >> 6);
  if (wid < 2) oproj_wave<5, 5>(o, wo, p, lds, tid, wid, nst);
  else if (wid < 4) oproj_wave<5, 4>(o, wo, p, lds, tid, wid, nst);
  else oproj_wave<4, 4>(o, wo, p, lds, tid, wid, nst);
}

__global__ __launch_bounds__(256) void oreduce_kernel(const float* __restrict__ part, u16* __restrict__ out) {
  const int idx = blockIdx.x * 256 + threadIdx.x;
  const int total = 16 * SQ * DM / 4;
  if (idx >= total) return;
  f32x4 s = *(const f32x4*)(part + (size_t)idx * 4);
#pragma unroll
  for (int k = 1; k < OP_SPLIT; ++k) s += *(const f32x4*)(part + (size_t)k * 16 * SQ * DM + (size_t)idx * 4);
  const int e = idx * 4, n = e % DM, row = (e / DM) % SQ, bb = e / (DM * SQ), blk = bb >> 3, b = bb & 7;
  u32x2 v = {cvtpk(s[0], s[1]), cvtpk(s[2], s[3])};
  *(u32x2*)(out + ((size_t)(b * SQ + row) * 2 + blk) * DM + n) = v;
}

constexpr int SLOT = 16384, NSLOT = 5, NTILE = SK / 32;
constexpr float THR = 6.0f;
#define ROLE_ROT 0
#define ROTF 0
#define WLOAD glds16c
#define XLOAD(p) (*(p))
#define KVAT -1
#define AMAP 0
#define XDIST 4
#define SCHED 1

template <int NB>
__device__ __forceinline__ void attn_wave(const u16* Xg, const u16* Wg, const u16* Kg, const u16* Vg, u16* __restrict__ Og,
                                          char* lds, int tid, int wid, int qb0) {
  const int lane = tid & 63, g = lane >> 4, i16 = lane & 15;
  lds_cptr l3 = (lds_cptr)lds;
  const unsigned lds0 = (unsigned)(uintptr_t)lds;
  const u16* src = (wid < 2) ? Kg : Vg;
  unsigned voff[4];
#pragma unroll
  for (int it = 0; it < 4; ++it) {
    const int row = (wid & 1) * 16 + it * 4 + g;
    const int ch = (wid < 2) ? (i16 ^ (row & 15)) : (i16 ^ ((row & 7) << 1));
    voff[it] = row * 256 + ch * 16;
  }
  const unsigned ldst = lds0 + wid * 4096;
#define ISSUE1(t, slot, it) glds16(src, voff[it] + (unsigned)(t) * 8192u, (unsigned)__builtin_amdgcn_readfirstlane(ldst + (slot) * SLOT + (it) * 1024))
#define ISSUE(t, slot) do { ISSUE1(t, slot, 0); ISSUE1(t, slot, 1); ISSUE1(t, slot, 2); ISSUE1(t, slot, 3); } while (0)
  bf16x8 qf[NB][4];
  {
    constexpr int XD = XDIST, NST = DM / 32;
    const unsigned wbase = lds0 + 3 * SLOT;
    unsigned woff[2];
#pragma unroll
    for (int it = 0; it < 2; ++it) {
      const int row = wid * 8 + it * 4 + g;
      const int f = (row & 3) | ((row >> 1) & 4);
      woff[it] = row * (HD * 2) + ((i16 ^ (f << 1)) << 4);
    }
#define XCNT(j) ((((j) & 1) == 0 && (j) + XD < NST) ? 2 * NB : 0)
#define WISSUE(c) do { _Pragma("unroll") for (int it = 0; it < 2; ++it) \
      glds16c(Wg, woff[it] + (unsigned)(c) * (32u * HD * 2u), (unsigned)__builtin_amdgcn_readfirstlane(wbase + ((c) & 3) * 8192 + wid * 2048 + it * 1024)); } while (0)
#define XLD(c) do { _Pragma("unroll") for (int qb = 0; qb < NB; ++qb) { \
      xf[c][qb] = *(const bf16x8*)(xp[qb] + (c) * 64); xf[(c) + 1][qb] = *(const bf16x8*)(xp[qb] + (c) * 64 + 64); } } while (0)
    const char* xp[NB];
#pragma unroll
    for (int qb = 0; qb < NB; ++qb) xp[qb] = (const char*)Xg + ((qb0 + qb) * 16 + i16) * (DM * 2) + g * 16;
    bf16x8 xf[NST][NB];
    f32x4 acc[NB][8];
#pragma unroll
    for (int qb = 0; qb < NB; ++qb)
#pragma unroll
      for (int db = 0; db < 8; ++db) acc[qb][db] = f32x4{0.f, 0.f, 0.f, 0.f};
    WISSUE(0); WISSUE(1); WISSUE(2); WISSUE(3);
#pragma unroll
    for (int c = 0; c < XD; c += 2) XLD(c);
    ISSUE(0, 0); ISSUE(1, 1); ISSUE(2, 2);
    const int wr = 8 * g + (i16 >> 2);
    const int wrd = 3 * SLOT + wr * 256 + ((((wr & 3) | ((wr >> 1) & 4))) << 5) + (lane & 3) * 8;
    bf16x8 wf[8];
#define WRD(db, c_) do { const s16x4 lo = vtr(l3 + ((c_) & 3) * 8192 + (wrd ^ ((db) << 5))); const s16x4 hi = vtr(l3 + ((c_) & 3) * 8192 + (wrd ^ ((db) << 5)) + 1024); \
        wf[db] = (bf16x8){lo[0], lo[1], lo[2], lo[3], hi[0], hi[1], hi[2], hi[3]}; } while (0)
    asm volatile("s_waitcnt vmcnt(%0)\n\ts_barrier" :: "i"(NB * XD + (KVAT < 0 ? 12 : 0)) : "memory");
#pragma unroll
    for (int db = 0; db < 8; ++db) WRD(db, 0);
#pragma unroll
    for (int c = 0; c < NST; ++c) {
      if (c < 3) asm volatile("s_waitcnt lgkmcnt(0)\n\ts_barrier" ::: "memory");
      else if (c + 1 < NST) asm volatile("s_waitcnt vmcnt(%0) lgkmcnt(0)\n\ts_barrier" :: "i"(XCNT(c - 3) + ((c + 2 < NST) ? 2 : 0) + XCNT(c - 2) + ((c + 3 < NST) ? 2 : 0) + XCNT(c - 1)) : "memory");
      if (c == KVAT) { ISSUE(0, 0); ISSUE(1, 1); ISSUE(2, 2); }
      if (c + 4 < NST) WISSUE(c + 4);
      SBAR();
#pragma unroll
      for (int db = 0; db < 8; ++db) {
#pragma unroll
        for (int qb = 0; qb < NB; ++qb) acc[qb][db] = MFMA16(wf[db], xf[c][qb], acc[qb][db]);
        if (c + 1 < NST) WRD(db, c + 1);
        SBAR();
      }
      if ((c & 1) == 0 && c + XD < NST) XLD(c + XD);
      SBAR();

    }
#undef WRD
#undef WISSUE
    asm volatile("s_waitcnt lgkmcnt(0)\n\ts_barrier" ::: "memory");
    ISSUE(3, 3);
    char* scr = lds + 4 * SLOT + wid * 4096;
#pragma unroll
    for (int qb = 0; qb < NB; ++qb) {
#pragma unroll
      for (int db = 0; db < 8; ++db) {
        u32x2 v = {cvtpk(acc[qb][db][0] * QSCALE, acc[qb][db][1] * QSCALE), cvtpk(acc[qb][db][2] * QSCALE, acc[qb][db][3] * QSCALE)};
        *(u32x2*)(scr + i16 * 256 + (((db * 2 + (g >> 1)) ^ i16) << 4) + (g & 1) * 8) = v;
      }
      asm volatile("s_waitcnt lgkmcnt(0)" ::: "memory");
#pragma unroll
      for (int ks = 0; ks < 4; ++ks) qf[qb][ks] = *(const bf16x8*)(scr + i16 * 256 + (((ks * 4 + g) ^ i16) << 4));
      asm volatile("s_waitcnt lgkmcnt(0)" ::: "memory");
#pragma unroll
      for (int ks = 0; ks < 4; ++ks) PIN(qf[qb][ks]);
    }
  }

  f32x4 o[NB][8], ls[NB], negm[NB];
#pragma unroll
  for (int qb = 0; qb < NB; ++qb) {
#pragma unroll
    for (int db = 0; db < 8; ++db) o[qb][db] = f32x4{0.f, 0.f, 0.f, 0.f};
    ls[qb] = f32x4{0.f, 0.f, 0.f, 0.f};
    negm[qb] = f32x4{0.f, 0.f, 0.f, 0.f};
  }
  const bf16x8 ones = {0x3F80, 0x3F80, 0x3F80, 0x3F80, 0x3F80, 0x3F80, 0x3F80, 0x3F80};
  const int kbase = i16 * 256 + ((g ^ i16) << 4);
  const int vrow = 4 * g + (i16 >> 2);
  const int vbase = 8192 + vrow * 256 + ((vrow & 7) << 5) + (lane & 3) * 8;
  f32x4 s[2][NB];
  bf16x8 pf[NB], pn[NB];
  bf16x8 kf[8], vf[8];

#define KRD(i, so_) kf[i] = ldsb128(l3 + (so_) + (kbase ^ (((i) >> 1) << 6)) + ((i) & 1) * 4096)
#define VRD(db, so_) do { const s16x4 lo = vtr(l3 + (so_) + (vbase ^ ((db) << 5))); const s16x4 hi = vtr(l3 + (so_) + (vbase ^ ((db) << 5)) + 4096); \
      vf[db] = (bf16x8){lo[0], lo[1], lo[2], lo[3], hi[0], hi[1], hi[2], hi[3]}; } while (0)
#define QKI(i) do { _Pragma("unroll") for (int qb = 0; qb < NB; ++qb) s[(i) & 1][qb] = MFMA16(kf[i], qf[qb][(i) >> 1], ((i) >> 1) == 0 ? negm[qb] : s[(i) & 1][qb]); } while (0)
#define ROWMAX(qb) fmx(fmx(fmx(fmx(s[0][qb][0], s[0][qb][1]), s[0][qb][2]), fmx(fmx(s[0][qb][3], s[1][qb][0]), s[1][qb][1])), fmx(s[1][qb][2], s[1][qb][3]))
#define XLANE_MAX(bm) do { \
        { auto r = __builtin_amdgcn_permlane16_swap(__float_as_uint(bm), __float_as_uint(bm), false, false); bm = fmx(__uint_as_float(r[0]), __uint_as_float(r[1])); } \
        { auto r = __builtin_amdgcn_permlane32_swap(__float_as_uint(bm), __float_as_uint(bm), false, false); bm = fmx(__uint_as_float(r[0]), __uint_as_float(r[1])); } } while (0)
#define EXPP(pi) do { unsigned w_ = cvtpk(__builtin_amdgcn_exp2f(s[((pi) >> 1) & 1][(pi) >> 2][((pi) & 1) * 2]), __builtin_amdgcn_exp2f(s[((pi) >> 1) & 1][(pi) >> 2][((pi) & 1) * 2 + 1])); \
      PIN(w_); pw[(pi) >> 2][(pi) & 3] = w_; } while (0)
#define PVI(db) do { _Pragma("unroll") for (int qb = 0; qb < NB; ++qb) o[qb][db] = MFMA16(vf[db], pf[qb], o[qb][db]); } while (0)
#define LSUM() do { _Pragma("unroll") for (int qb = 0; qb < NB; ++qb) ls[qb] = MFMA16(ones, pf[qb], ls[qb]); } while (0)

  asm volatile("s_waitcnt vmcnt(4) lgkmcnt(0)\n\ts_barrier" ::: "memory");
  u32x4 pw[NB];
#pragma unroll
  for (int i = 0; i < 8; ++i) KRD(i, 0);
#pragma unroll
  for (int i = 0; i < 8; ++i) QKI(i);
#pragma unroll
  for (int qb = 0; qb < NB; ++qb) {
    float bm = ROWMAX(qb); XLANE_MAX(bm);
    negm[qb] -= bm; s[0][qb] -= bm; s[1][qb] -= bm;
  }
#pragma unroll
  for (int pi = 0; pi < 4 * NB; ++pi) EXPP(pi);
#pragma unroll
  for (int qb = 0; qb < NB; ++qb) pf[qb] = __builtin_bit_cast(bf16x8, pw[qb]);
  KRD(0, SLOT); KRD(1, SLOT);
  int sprev = 0, scur = 1;
  for (int t = 1; t < NTILE; ++t) {
    const int snext = (scur == NSLOT - 1) ? 0 : scur + 1;
    const int so = scur * SLOT, sp = sprev * SLOT;
    SBAR();
#pragma unroll
    for (int i = 0; i < 8; ++i) {
      if (i + 2 < 8) KRD(i + 2, so);
      else VRD(i - 6, sp);
      QKI(i);
      SBAR();
    }
    float mx[NB]; bool trig = false;
#pragma unroll
    for (int qb = 0; qb < NB; ++qb) { mx[qb] = ROWMAX(qb); trig |= (mx[qb] > THR); }
    float al[NB]; bool resc = false;
    if (__builtin_expect(__any(trig), 0)) {
      resc = true;
#pragma unroll
      for (int qb = 0; qb < NB; ++qb) {
        float bm = mx[qb]; XLANE_MAX(bm);
        const float d = fmx(bm, 0.f);
        negm[qb] -= d; s[0][qb] -= d; s[1][qb] -= d;
        al[qb] = __builtin_amdgcn_exp2f(-d);
      }
    }
    if (t + 2 < NTILE) asm volatile("s_waitcnt vmcnt(4)\n\ts_barrier" ::: "memory");
    else if (t + 1 < NTILE) asm volatile("s_waitcnt vmcnt(0)\n\ts_barrier" ::: "memory");
    const int ns = (sprev == 0) ? (NSLOT - 1) : (sprev - 1);
    const bool more = (t + 3 < NTILE);
    SBAR();
    LSUM();
#pragma unroll
    for (int db = 0; db < 8; ++db) {
      if (db + 2 < 8) VRD(db + 2, sp);
      else if (t + 1 < NTILE) KRD(db - 6, snext * SLOT);
      PVI(db);
      if (db < 4 && more) ISSUE1(t + 3, ns, db);
      if (NB == 3) { EXPP((3 * db) >> 1); if (db & 1) EXPP(((3 * db) >> 1) + 1); }
      else EXPP(db);
      SBAR();
    }
#pragma unroll
    for (int qb = 0; qb < NB; ++qb) pf[qb] = __builtin_bit_cast(bf16x8, pw[qb]);
    if (__builtin_expect(resc, 0)) {
#pragma unroll
      for (int qb = 0; qb < NB; ++qb) {
#pragma unroll
        for (int db = 0; db < 8; ++db) o[qb][db] *= al[qb];
        ls[qb] *= al[qb];
      }
    }
    sprev = scur; scur = snext;
  }
  LSUM();
#pragma unroll
  for (int db = 0; db < 8; ++db) { VRD(db, sprev * SLOT); PVI(db); }
#undef ISSUE
#pragma unroll
  for (int qb = 0; qb < NB; ++qb) {
    const float inv = 1.0f / ls[qb][0];
#pragma unroll
    for (int db = 0; db < 8; ++db) {
      u32x2 v = {cvtpk(o[qb][db][0] * inv, o[qb][db][1] * inv), cvtpk(o[qb][db][2] * inv, o[qb][db][3] * inv)};
      *(u32x2*)(Og + (size_t)((db >> 2) * SQ + (qb0 + qb) * 16 + i16) * 64 + (db & 3) * 16 + g * 4) = v;
    }
  }
}

__global__ __launch_bounds__(256, 2) void attn_kernel(const u16* __restrict__ xA, const u16* __restrict__ xB, const u16* __restrict__ wqA, const u16* __restrict__ wqB,
                                                      const u16* __restrict__ kA, const u16* __restrict__ vA,
                                                      const u16* __restrict__ kB, const u16* __restrict__ vB, u16* __restrict__ ows) {
  extern __shared__ __attribute__((aligned(16))) char lds[];
  const int item = blockIdx.x, blk = item >> 8, bh = item & 255, b = bh >> 5, h = bh & 31;
  const u16* K = (blk ? kB : kA) + (size_t)bh * SK * DH;
  const u16* V = (blk ? vB : vA) + (size_t)bh * SK * DH;
  const u16* X = (blk ? xB : xA) + (size_t)b * SQ * DM;
  const u16* W = (blk ? wqB : wqA) + h * DH;
  u16* O = ows + ((size_t)(blk * 8 + b) * SQ) * HD + (size_t)h * SQ * DH;
  const int tid = threadIdx.x, wid = __builtin_amdgcn_readfirstlane(tid >> 6);
  const int role = (wid + ROLE_ROT * blk) & 3;
  if (role == 0) attn_wave<3>(X, W, K, V, O, lds, tid, wid, 0);
  else attn_wave<2>(X, W, K, V, O, lds, tid, wid, 1 + 2 * role);
}

extern "C" void kernel_launch(void* const* d_in, const int* in_sizes, int n_in, void* d_out, int out_size, void* d_ws,
                              size_t ws_size, hipStream_t stream) {
  const u16* xA = (const u16*)d_in[0]; const u16* kA = (const u16*)d_in[1]; const u16* vA = (const u16*)d_in[2];
  const u16* wqA = (const u16*)d_in[3]; const u16* woA = (const u16*)d_in[4];
  const u16* xB = (const u16*)d_in[5]; const u16* kB = (const u16*)d_in[6]; const u16* vB = (const u16*)d_in[7];
  const u16* wqB = (const u16*)d_in[8]; const u16* woB = (const u16*)d_in[9];
  char* ws = (char*)d_ws;
  u16* qws = (u16*)ws;
  u16* ows = (u16*)(ws + (size_t)512 * SQ * DH * 2);
  float* part = (float*)(ws + (size_t)2 * 512 * SQ * DH * 2);
  hipFuncSetAttribute((const void*)attn_kernel, hipFuncAttributeMaxDynamicSharedMemorySize, NSLOT * SLOT);
  attn_kernel<<<512, 256, NSLOT * SLOT, stream>>>(xA, xB, wqA, wqB, kA, vA, kB, vB, ows);
  hipFuncSetAttribute((const void*)oproj_kernel, hipFuncAttributeMaxDynamicSharedMemorySize, OP_NSTG * OP_STAGE);
  oproj_kernel<<<240, 512, OP_NSTG * OP_STAGE, stream>>>(ows, woA, woB, part);
  oreduce_kernel<<<(16 * SQ * DM / 4 + 255) / 256, 256, 0, stream>>>(part, (u16*)d_out);
}
```

```cpp
#include <hip/hip_runtime.h>
#include <hip/hip_bf16.h>
#include <stdint.h>

constexpr int NB_ = 8, SQ = 144, DM = 640, NH = 32, SK = 1024, DH = 128;
constexpr int HD = NH * DH;
constexpr int MROWS = NB_ * SQ;
constexpr float QSCALE = 0.088388347648318447f * 1.4426950408889634f;

using bf16x8 = __attribute__((ext_vector_type(8))) short;
using s16x4 = __attribute__((ext_vector_type(4))) short;
using f32x4 = __attribute__((ext_vector_type(4))) float;
using u32x4 = __attribute__((ext_vector_type(4))) unsigned;
using u32x2 = __attribute__((ext_vector_type(2))) unsigned;
typedef short v4i16_t __attribute__((ext_vector_type(4)));
typedef __attribute__((address_space(3))) const char* lds_cptr;
typedef unsigned short u16;

#define SBAR() __builtin_amdgcn_sched_barrier(0)
#define PIN(x) asm volatile("" : "+v"(x))
#define MFMA16(a, b, c) __builtin_amdgcn_mfma_f32_16x16x32_bf16(a, b, c, 0, 0, 0)

typedef __bf16 bf16x2_t __attribute__((ext_vector_type(2)));
typedef float f32x2_t __attribute__((ext_vector_type(2)));
__device__ __forceinline__ unsigned cvtpk(float lo, float hi) {
  const f32x2_t v = {lo, hi};
  return __builtin_bit_cast(unsigned, __builtin_convertvector(v, bf16x2_t));
}
__device__ __forceinline__ s16x4 vtr(lds_cptr p) {
  return __builtin_bit_cast(s16x4, __builtin_amdgcn_ds_read_tr16_b64_v4i16((__attribute__((address_space(3))) v4i16_t*)p));
}
__device__ __forceinline__ bf16x8 ldsb128(lds_cptr p) {
  return *(const __attribute__((address_space(3))) bf16x8*)p;
}
__device__ __forceinline__ float fmx(float a, float b) { return __builtin_elementwise_maximum(a, b); }
__device__ __forceinline__ void glds16c(const void* sbase, unsigned voff, unsigned lds_base) {
  unsigned sv;
  asm volatile("s_mov_b32 %0, m0\n\ts_mov_b32 m0, %3\n\ts_nop 0\n\tglobal_load_lds_dwordx4 %1, %2\n\ts_mov_b32 m0, %0"
               : "=&s"(sv) : "v"(voff), "s"(sbase), "s"(lds_base) : "memory");
}
__device__ __forceinline__ void glds16(const void* sbase, unsigned voff, unsigned lds_base) {
  unsigned sv;
  asm volatile("s_mov_b32 %0, m0\n\ts_mov_b32 m0, %3\n\ts_nop 0\n\tglobal_load_lds_dwordx4 %1, %2 nt\n\ts_mov_b32 m0, %0"
               : "=&s"(sv) : "v"(voff), "s"(sbase), "s"(lds_base) : "memory");
}

template <int NB>
__device__ __forceinline__ void gemm_simple(const u16* __restrict__ W, int ldw, const u16* __restrict__ X, int ldx,
                                            int nchunks, char* lds, f32x4 (&acc)[NB][8], int tid) {
  const int lane = tid & 63, g = lane >> 4, i16 = lane & 15;
  lds_cptr l3 = (lds_cptr)lds;
  for (int kc = 0; kc < nchunks; ++kc) {
    __syncthreads();
#pragma unroll
    for (int it = 0; it < 4; ++it) {
      const int p = it * 256 + tid, row = p >> 4, ch = p & 15;
      const u32x4 v = *(const u32x4*)(W + (size_t)(kc * 64 + row) * ldw + ch * 8);
      const int f = (row & 3) | ((row >> 1) & 4);
      *(u32x4*)(lds + row * 256 + ((ch ^ (f << 1)) << 4)) = v;
    }
    __syncthreads();
#pragma unroll
    for (int kk = 0; kk < 2; ++kk) {
      bf16x8 xb[NB];
#pragma unroll
      for (int qb = 0; qb < NB; ++qb)
        xb[qb] = *(const bf16x8*)(X + (size_t)(qb * 16 + i16) * ldx + kc * 64 + kk * 32 + g * 8);
#pragma unroll
      for (int db = 0; db < 8; ++db) {
        const int r0 = kk * 32 + 8 * g + (i16 >> 2);
        const int f0 = (r0 & 3) | ((r0 >> 1) & 4);
        const int a0 = r0 * 256 + ((db ^ f0) << 5) + (lane & 3) * 8;
        const s16x4 lo = vtr(l3 + a0);
        const s16x4 hi = vtr(l3 + a0 + 4 * 256);
        const bf16x8 wf = {lo[0], lo[1], lo[2], lo[3], hi[0], hi[1], hi[2], hi[3]};
#pragma unroll
        for (int qb = 0; qb < NB; ++qb) acc[qb][db] = MFMA16(wf, xb[qb], acc[qb][db]);
      }
    }
  }
}

constexpr int QP_STAGE = 18432 + 32768, QP_NSTG = 3;
template <int MB, int NDMA>
__device__ __forceinline__ void qproj_wave(const u16* X, const u16* W, u16* __restrict__ qo, char* lds, int tid, int wid) {
  const int lane = tid & 63, g = lane >> 4, i16 = lane & 15;
  lds_cptr l3 = (lds_cptr)lds;
  const unsigned lds0 = (unsigned)(uintptr_t)lds;
  const int ng = wid & 3, mb0 = (wid >> 2) * 5;
  constexpr int NST = DM / 64;
  const int xrow = wid * 8 + (lane >> 3), wrow = wid * 2 + (lane >> 5);
  const unsigned xoff = xrow * (DM * 2) + (((lane & 7) ^ (xrow & 7)) << 4);
  unsigned woff[4];
#pragma unroll
  for (int it = 0; it < 4; ++it) { const int row = wrow + 16 * it; woff[it] = row * (HD * 2) + (((lane & 31) ^ (((row & 3) | ((row >> 1) & 4)) << 1)) << 4); }
#define QISSUE(s_) do { const unsigned sb_ = lds0 + ((s_) % QP_NSTG) * QP_STAGE; const char* xs_ = (const char*)X + (s_) * 128; const char* ws_ = (const char*)W + (size_t)(s_) * (64 * HD * 2); \
    glds16c(xs_, xoff, (unsigned)__builtin_amdgcn_readfirstlane(sb_ + wid * 1024)); \
    glds16c(xs_ + 64 * DM * 2, xoff, (unsigned)__builtin_amdgcn_readfirstlane(sb_ + (wid + 8) * 1024)); \
    if (NDMA == 7) glds16c(xs_ + 128 * DM * 2, xoff, (unsigned)__builtin_amdgcn_readfirstlane(sb_ + (wid + 16) * 1024)); \
    _Pragma("unroll") for (int it = 0; it < 4; ++it) glds16c(ws_, woff[it], (unsigned)__builtin_amdgcn_readfirstlane(sb_ + 18432 + (wid + 8 * it) * 1024)); } while (0)
  f32x4 acc[4][MB];
#pragma unroll
  for (int a = 0; a < 4; ++a)
#pragma unroll
    for (int b = 0; b < MB; ++b) acc[a][b] = f32x4{0.f, 0.f, 0.f, 0.f};
  QISSUE(0); QISSUE(1);
  const int xrd = (mb0 * 16 + i16) * 128 + ((g ^ (i16 & 7)) << 4);
  const int wr = 8 * g + (i16 >> 2);
  const int wrd = 18432 + wr * 512 + ((((wr & 3) | ((wr >> 1) & 4))) << 5) + (lane & 3) * 8;
#pragma unroll
  for (int s = 0; s < NST; ++s) {
    if (s + 1 < NST) asm volatile("s_waitcnt vmcnt(%0) lgkmcnt(0)\n\ts_barrier" :: "i"(NDMA) : "memory");
    else asm volatile("s_waitcnt vmcnt(0) lgkmcnt(0)\n\ts_barrier" ::: "memory");
    if (s + 2 < NST) QISSUE(s + 2);
    const int so = (s % QP_NSTG) * QP_STAGE;
#pragma unroll
    for (int kk = 0; kk < 2; ++kk) {
      bf16x8 wf[4], xf[MB];
#pragma unroll
      for (int nb = 0; nb < 4; ++nb) {
        const int db = 4 * (ng & 1) + nb;
        const s16x4 lo = vtr(l3 + so + (wrd ^ (db << 5)) + (ng >> 1) * 256 + kk * 16384);
        const s16x4 hi = vtr(l3 + so + (wrd ^ (db << 5)) + (ng >> 1) * 256 + kk * 16384 + 2048);
        wf[nb] = (bf16x8){lo[0], lo[1], lo[2], lo[3], hi[0], hi[1], hi[2], hi[3]};
      }
#pragma unroll
      for (int mb = 0; mb < MB; ++mb) xf[mb] = ldsb128(l3 + so + (xrd ^ (kk << 6)) + mb * 2048);
#pragma unroll
      for (int mb = 0; mb < MB; ++mb)
#pragma unroll
        for (int nb = 0; nb < 4; ++nb) acc[nb][mb] = MFMA16(wf[nb], xf[mb], acc[nb][mb]);
    }
  }
#undef QISSUE
  u16* q = qo + (size_t)(ng >> 1) * SQ * DH;
#pragma unroll
  for (int mb = 0; mb < MB; ++mb)
#pragma unroll
    for (int nb = 0; nb < 4; ++nb) {
      u32x2 v = {cvtpk(acc[nb][mb][0] * QSCALE, acc[nb][mb][1] * QSCALE), cvtpk(acc[nb][mb][2] * QSCALE, acc[nb][mb][3] * QSCALE)};
      *(u32x2*)(q + (size_t)((mb0 + mb) * 16 + i16) * DH + (4 * (ng & 1) + nb) * 16 + g * 4) = v;
    }
}

__global__ __launch_bounds__(512, 2) void qproj_kernel(const u16* __restrict__ xA, const u16* __restrict__ xB,
                                                       const u16* __restrict__ wqA, const u16* __restrict__ wqB,
                                                       u16* __restrict__ qws) {
  extern __shared__ __attribute__((aligned(16))) char lds[];
  const int xcd = blockIdx.x & 7, jj = blockIdx.x >> 3, hp = xcd * 2 + (jj & 1), b = (jj >> 1) & 7, blk = jj >> 4;
  const u16* x = (blk ? xB : xA) + (size_t)b * SQ * DM;
  const u16* wq = (blk ? wqB : wqA) + hp * 256;
  u16* qo = qws + (size_t)(blk * 256 + b * 32 + hp * 2) * SQ * DH;
  const int tid = threadIdx.x, wid = __builtin_amdgcn_readfirstlane(tid >> 6);
  if (wid < 2) qproj_wave<5, 7>(x, wq, qo, lds, tid, wid);
  else if (wid < 4) qproj_wave<5, 6>(x, wq, qo, lds, tid, wid);
  else qproj_wave<4, 6>(x, wq, qo, lds, tid, wid);
}

#define OXL glds16c
#define OWL glds16c
#define OROT ((3 * (r / 5) + 4 * nt) % 21)
#define QROT 0
constexpr int OP_STAGE = 18432 + 16384, OP_NSTG = 4, OP_SPLIT = 3;
template <int MB, int NDMA>
__device__ __forceinline__ void oproj_wave(const u16* X, const u16* W, float* __restrict__ part, char* lds, int tid, int wid, int nst, int rot) {
  const int lane = tid & 63, g = lane >> 4, i16 = lane & 15;
  lds_cptr l3 = (lds_cptr)lds;
  const unsigned lds0 = (unsigned)(uintptr_t)lds;
  const int kk = wid & 1, ng = (wid >> 1) & 1, mb0 = (wid >> 2) * 5;
  const int xrow = wid * 8 + (lane >> 3), wrow = wid * 4 + g;
  const unsigned xoff = xrow * 128 + (((lane & 7) ^ (xrow & 7)) << 4);
  const unsigned woff = wrow * (DM * 2) + ((i16 ^ (((wrow & 3) | ((wrow >> 1) & 4)) << 1)) << 4);
#define OISSUE(s_) do { const unsigned sb_ = lds0 + ((s_) & 3) * OP_STAGE; const int sr_ = ((s_) + rot >= nst) ? (s_) + rot - nst : (s_) + rot; \
    const char* xs_ = (const char*)X + (size_t)sr_ * 18432; const char* ws_ = (const char*)W + (size_t)sr_ * (64 * DM * 2); \
    OXL(xs_, xoff, (unsigned)__builtin_amdgcn_readfirstlane(sb_ + wid * 1024)); \
    OXL(xs_ + 8192, xoff, (unsigned)__builtin_amdgcn_readfirstlane(sb_ + (wid + 8) * 1024)); \
    if (NDMA == 5) OXL(xs_ + 16384, xoff, (unsigned)__builtin_amdgcn_readfirstlane(sb_ + (wid + 16) * 1024)); \
    OWL(ws_, woff, (unsigned)__builtin_amdgcn_readfirstlane(sb_ + 18432 + wid * 1024)); \
    OWL(ws_ + 32 * DM * 2, woff, (unsigned)__builtin_amdgcn_readfirstlane(sb_ + 18432 + (wid + 8) * 1024)); } while (0)
  f32x4 acc[4][MB];
#pragma unroll
  for (int a = 0; a < 4; ++a)
#pragma unroll
    for (int b = 0; b < MB; ++b) acc[a][b] = f32x4{0.f, 0.f, 0.f, 0.f};
  OISSUE(0); OISSUE(1); OISSUE(2); OISSUE(3);
  const int xrd = (mb0 * 16 + i16) * 128 + (((4 * kk + g) ^ (i16 & 7)) << 4);
  const int wr = 8 * g + (i16 >> 2);
  const int wrd = 18432 + kk * 8192 + wr * 256 + ((((wr & 3) | ((wr >> 1) & 4))) << 5) + (lane & 3) * 8;
  bf16x8 wf[4], wn[4], xf[MB];
#define OWRD(dst, nb, so_) do { const s16x4 lo = vtr(l3 + (so_) + (wrd ^ ((4 * ng + (nb)) << 5))); const s16x4 hi = vtr(l3 + (so_) + (wrd ^ ((4 * ng + (nb)) << 5)) + 1024); \
      dst = (bf16x8){lo[0], lo[1], lo[2], lo[3], hi[0], hi[1], hi[2], hi[3]}; } while (0)
  asm volatile("s_waitcnt vmcnt(%0)\n\ts_barrier" :: "i"(3 * NDMA) : "memory");
#pragma unroll
  for (int nb = 0; nb < 4; ++nb) OWRD(wf[nb], nb, 0);
#pragma unroll
  for (int mb = 0; mb < MB; ++mb) xf[mb] = ldsb128(l3 + xrd + mb * 2048);
  for (int s = 0; s < nst; ++s) {
    if (s + 3 < nst) asm volatile("s_waitcnt vmcnt(%0) lgkmcnt(0)\n\ts_barrier" :: "i"(2 * NDMA) : "memory");
    else if (s + 2 < nst) asm volatile("s_waitcnt vmcnt(%0) lgkmcnt(0)\n\ts_barrier" :: "i"(NDMA) : "memory");
    else asm volatile("s_waitcnt vmcnt(0) lgkmcnt(0)\n\ts_barrier" ::: "memory");
    if (s + 4 < nst) OISSUE(s + 4);
    const int sn = (s + 1 < nst) ? ((s + 1) & 3) * OP_STAGE : (s & 3) * OP_STAGE;
    SBAR();
#pragma unroll
    for (int mb = 0; mb < MB; ++mb) {
      if (mb < 4) OWRD(wn[mb], mb, sn);
#pragma unroll
      for (int nb = 0; nb < 4; ++nb) acc[nb][mb] = MFMA16(wf[nb], xf[mb], acc[nb][mb]);
      xf[mb] = ldsb128(l3 + sn + xrd + mb * 2048);
      SBAR();
    }
#pragma unroll
    for (int nb = 0; nb < 4; ++nb) wf[nb] = wn[nb];
  }
#undef OISSUE
  asm volatile("s_waitcnt lgkmcnt(0)\n\ts_barrier" ::: "memory");
  char* red = lds + (wid >> 1) * 20480 + lane * 16;
  if (kk == 1) {
#pragma unroll
    for (int mb = 0; mb < MB; ++mb)
#pragma unroll
      for (int nb = 0; nb < 4; ++nb) *(f32x4*)(red + (mb * 4 + nb) * 1024) = acc[nb][mb];
  }
  asm volatile("s_waitcnt lgkmcnt(0)\n\ts_barrier" ::: "memory");
  if (kk == 0) {
#pragma unroll
    for (int mb = 0; mb < MB; ++mb)
#pragma unroll
      for (int nb = 0; nb < 4; ++nb) {
        const f32x4 v = acc[nb][mb] + *(const f32x4*)(red + (mb * 4 + nb) * 1024);
        *(f32x4*)(part + (size_t)((mb0 + mb) * 16 + i16) * DM + (4 * ng + nb) * 16 + g * 4) = v;
      }
  }
}

__device__ __forceinline__ void oproj_touch(const u16* X, const u16* W, unsigned dummy, int lane, int nst, int nt, int b) {
  const unsigned xo = lane * 16 + nt * 1024;
  const unsigned wo = ((b & 3) * 16 + (lane >> 4)) * (DM * 2) + (lane & 15) * 16;
  for (int s = 0; s < nst; ++s) {
    const char* xs = (const char*)X + (size_t)s * 18432;
    const char* ws = (const char*)W + (size_t)s * 64 * DM * 2;
    glds16c(xs, xo, dummy); glds16c(xs + 5 * 1024, xo, dummy); glds16c(xs + 10 * 1024, xo, dummy);
    if (nt < 3) glds16c(xs + 15 * 1024, xo, dummy);
    glds16c(ws, wo, dummy); glds16c(ws + 4 * DM * 2, wo, dummy); glds16c(ws + 8 * DM * 2, wo, dummy); glds16c(ws + 12 * DM * 2, wo, dummy);
    asm volatile("s_waitcnt vmcnt(40)" ::: "memory");
  }
  asm volatile("s_waitcnt vmcnt(0)" ::: "memory");
}

__global__ __launch_bounds__(512, 2) void oproj_kernel(const u16* __restrict__ ows, const u16* __restrict__ woA,
                                                       const u16* __restrict__ woB, float* __restrict__ part) {
  extern __shared__ __attribute__((aligned(16))) char lds[];
  const int L = (blockIdx.x & 7) * 30 + (blockIdx.x >> 3), grp = L / 40, r = L % 40, blk = grp / 3, ksp = grp % 3, nt = r % 5, bb = blk * 8 + r / 5;
  const int s0 = ksp == 0 ? 0 : (ksp == 1 ? 22 : 43), nst = ksp == 0 ? 22 : 21;
  const u16* o = ows + (size_t)bb * SQ * HD + (size_t)s0 * SQ * 64;
  const u16* wo = (blk ? woB : woA) + (size_t)s0 * 64 * DM + nt * 128;
  float* p = part + ((size_t)ksp * 16 + bb) * SQ * DM + nt * 128;
  const int tid = threadIdx.x, wid = __builtin_amdgcn_readfirstlane(tid >> 6);
  const int rot = OROT;
  if (wid < 2) oproj_wave<5, 5>(o, wo, p, lds, tid, wid, nst, rot);
  else if (wid < 4) oproj_wave<5, 4>(o, wo, p, lds, tid, wid, nst, rot);
  else if (wid < 8) oproj_wave<4, 4>(o, wo, p, lds, tid, wid, nst, rot);

}

__global__ __launch_bounds__(256) void oreduce_kernel(const float* __restrict__ part, u16* __restrict__ out) {
  const int idx = blockIdx.x * 256 + threadIdx.x;
  const int total = 16 * SQ * DM / 4;
  if (idx >= total) return;
  f32x4 s = *(const f32x4*)(part + (size_t)idx * 4);
#pragma unroll
  for (int k = 1; k < OP_SPLIT; ++k) s += *(const f32x4*)(part + (size_t)k * 16 * SQ * DM + (size_t)idx * 4);
  const int e = idx * 4, n = e % DM, row = (e / DM) % SQ, bb = e / (DM * SQ), blk = bb >> 3, b = bb & 7;
  u32x2 v = {cvtpk(s[0], s[1]), cvtpk(s[2], s[3])};
  *(u32x2*)(out + ((size_t)(b * SQ + row) * 2 + blk) * DM + n) = v;
}

constexpr int SLOT = 16384, NSLOT = 5, NTILE = SK / 32;
constexpr float THR = 6.0f;
#define ROLE_ROT 0
#define ROTF 0
#define WLOAD glds16c
#define XLOAD(p) (*(p))
#define KVAT -1
#define AMAP 0
#define DELAY 0
#define FUSED 1
#define XDIST 4
#define SCHED 1

template <int NB, bool X5>
__device__ __forceinline__ void attn_wave(const u16* Xg, const u16* Wg, const u16* Kg, const u16* Vg, u16* __restrict__ Og,
                                          char* lds, int tid, int wid, int qb0) {
  const int lane = tid & 63, g = lane >> 4, i16 = lane & 15;
  lds_cptr l3 = (lds_cptr)lds;
  const unsigned lds0 = (unsigned)(uintptr_t)lds;
  const u16* src = (wid < 2) ? Kg : Vg;
  unsigned voff[4];
#pragma unroll
  for (int it = 0; it < 4; ++it) {
    const int row = (wid & 1) * 16 + it * 4 + g;
    const int ch = (wid < 2) ? (i16 ^ (row & 15)) : (i16 ^ ((row & 7) << 1));
    voff[it] = row * 256 + ch * 16;
  }
  const unsigned ldst = lds0 + wid * 4096;
#define ISSUE1(t, slot, it) glds16(src, voff[it] + (unsigned)(t) * 8192u, (unsigned)__builtin_amdgcn_readfirstlane(ldst + (slot) * SLOT + (it) * 1024))
#define ISSUE(t, slot) do { ISSUE1(t, slot, 0); ISSUE1(t, slot, 1); ISSUE1(t, slot, 2); ISSUE1(t, slot, 3); } while (0)
  bf16x8 qf[NB][4];
  {
    constexpr int NST = DM / 32, XB = 16384, XSZ = 18432, WB = 53248;
    const int xrow = wid * 8 + (lane >> 3);
    const unsigned xoff = xrow * (DM * 2) + (((lane & 7) ^ (xrow & 7)) << 4);
    unsigned woff[2];
#pragma unroll
    for (int it = 0; it < 2; ++it) {
      const int row = wid * 8 + it * 4 + g;
      woff[it] = row * (HD * 2) + ((i16 ^ (((row & 3) | ((row >> 1) & 4)) << 1)) << 4);
    }
    constexpr bool x5 = X5;
    const int qrot = QROT;
#define RS(s_) (((s_) + qrot >= NST / 2) ? (s_) + qrot - NST / 2 : (s_) + qrot)
#define WISSUE(c) do { const char* ws_ = (const char*)Wg + (size_t)(RS((c) >> 1) * 2 + ((c) & 1)) * (32 * HD * 2); _Pragma("unroll") for (int it = 0; it < 2; ++it) \
      glds16c(ws_, woff[it], (unsigned)__builtin_amdgcn_readfirstlane(lds0 + WB + ((c) % 3) * 8192 + wid * 2048 + it * 1024)); } while (0)
#define XISSUE(s_) do { const char* xs_ = (const char*)Xg + RS(s_) * 128; _Pragma("unroll") for (int it = 0; it < 4; ++it) \
      glds16c(xs_ + it * 32 * DM * 2, xoff, (unsigned)__builtin_amdgcn_readfirstlane(lds0 + XB + ((s_) & 1) * XSZ + (wid + 4 * it) * 1024)); \
      if (x5) glds16c(xs_ + (128 - 16) * DM * 2, xoff, (unsigned)__builtin_amdgcn_readfirstlane(lds0 + XB + ((s_) & 1) * XSZ + (wid + 14) * 1024)); } while (0)
#define VMW(n4, n5, tail) do { if (x5) asm volatile("s_waitcnt vmcnt(%0)" tail :: "i"(n5) : "memory"); else asm volatile("s_waitcnt vmcnt(%0)" tail :: "i"(n4) : "memory"); } while (0)
    f32x4 acc[NB][8];
#pragma unroll
    for (int qb = 0; qb < NB; ++qb)
#pragma unroll
      for (int db = 0; db < 8; ++db) acc[qb][db] = f32x4{0.f, 0.f, 0.f, 0.f};
    XISSUE(0); WISSUE(0); WISSUE(1); XISSUE(1); WISSUE(2);
    ISSUE(0, 0);
    const int wr = 8 * g + (i16 >> 2);
    const int wrd = WB + wr * 256 + ((((wr & 3) | ((wr >> 1) & 4))) << 5) + (lane & 3) * 8;
    const int xrd = XB + (qb0 * 16 + i16) * 128 + ((g ^ (i16 & 7)) << 4);
    bf16x8 wf[8], xa[NB], xb_[NB];
#define WRD(db, c_) do { const s16x4 lo = vtr(l3 + ((c_) % 3) * 8192 + (wrd ^ ((db) << 5))); const s16x4 hi = vtr(l3 + ((c_) % 3) * 8192 + (wrd ^ ((db) << 5)) + 1024); \
        wf[db] = (bf16x8){lo[0], lo[1], lo[2], lo[3], hi[0], hi[1], hi[2], hi[3]}; } while (0)
#define XRD(dst, c_) do { _Pragma("unroll") for (int qb = 0; qb < NB; ++qb) dst[qb] = ldsb128(l3 + (((c_) >> 1) & 1) * XSZ + (xrd ^ (((c_) & 1) << 6)) + qb * 2048); } while (0)
    VMW(4 + 6, 5 + 6, "\n\ts_barrier");
#pragma unroll
    for (int db = 0; db < 8; ++db) WRD(db, 0);
    XRD(xa, 0);
#pragma unroll
    for (int c = 0; c < NST; ++c) {
      if (c == 0) asm volatile("s_waitcnt lgkmcnt(0)\n\ts_barrier" ::: "memory");
      else if (c == 1) asm volatile("s_waitcnt vmcnt(6) lgkmcnt(0)\n\ts_barrier" ::: "memory");
      else if (c + 1 < NST) {
        if (c & 1) asm volatile("s_waitcnt vmcnt(%0) lgkmcnt(0)\n\ts_barrier" :: "i"((c + 2 < NST) ? 2 : 0) : "memory");
        else VMW(((c + 2 < NST) ? 2 : 0) + ((((c - 1) >> 1) + 2 < NST / 2) ? 4 : 0), ((c + 2 < NST) ? 2 : 0) + ((((c - 1) >> 1) + 2 < NST / 2) ? 5 : 0), " lgkmcnt(0)\n\ts_barrier");
      }
      if (c + 3 < NST) WISSUE(c + 3);
      if ((c & 1) && (c >> 1) + 2 < NST / 2) XISSUE((c >> 1) + 2);
      if (c + 1 < NST) { if (c & 1) XRD(xa, c + 1); else XRD(xb_, c + 1); }
      SBAR();
#pragma unroll
      for (int db = 0; db < 8; ++db) {
#pragma unroll
        for (int qb = 0; qb < NB; ++qb) acc[qb][db] = MFMA16(wf[db], (c & 1) ? xb_[qb] : xa[qb], acc[qb][db]);
        if (c + 1 < NST) WRD(db, c + 1);
        SBAR();
      }
    }
#undef WRD
#undef XRD
#undef WISSUE
#undef XISSUE
    asm volatile("s_waitcnt lgkmcnt(0)\n\ts_barrier" ::: "memory");
    ISSUE(1, 1); ISSUE(2, 2); ISSUE(3, 3);
    char* scr = lds + 4 * SLOT + wid * 4096;
#pragma unroll
    for (int qb = 0; qb < NB; ++qb) {
#pragma unroll
      for (int db = 0; db < 8; ++db) {
        u32x2 v = {cvtpk(acc[qb][db][0] * QSCALE, acc[qb][db][1] * QSCALE), cvtpk(acc[qb][db][2] * QSCALE, acc[qb][db][3] * QSCALE)};
        *(u32x2*)(scr + i16 * 256 + (((db * 2 + (g >> 1)) ^ i16) << 4) + (g & 1) * 8) = v;
      }
      asm volatile("s_waitcnt lgkmcnt(0)" ::: "memory");
#pragma unroll
      for (int ks = 0; ks < 4; ++ks) qf[qb][ks] = *(const bf16x8*)(scr + i16 * 256 + (((ks * 4 + g) ^ i16) << 4));
      asm volatile("s_waitcnt lgkmcnt(0)" ::: "memory");
#pragma unroll
      for (int ks = 0; ks < 4; ++ks) PIN(qf[qb][ks]);
    }
  }

  f32x4 o[NB][8], ls[NB], negm[NB];
#pragma unroll
  for (int qb = 0; qb < NB; ++qb) {
#pragma unroll
    for (int db = 0; db < 8; ++db) o[qb][db] = f32x4{0.f, 0.f, 0.f, 0.f};
    ls[qb] = f32x4{0.f, 0.f, 0.f, 0.f};
    negm[qb] = f32x4{0.f, 0.f, 0.f, 0.f};
  }
  const bf16x8 ones = {0x3F80, 0x3F80, 0x3F80, 0x3F80, 0x3F80, 0x3F80, 0x3F80, 0x3F80};
  const int kbase = i16 * 256 + ((g ^ i16) << 4);
  const int vrow = 4 * g + (i16 >> 2);
  const int vbase = 8192 + vrow * 256 + ((vrow & 7) << 5) + (lane & 3) * 8;
  f32x4 s[2][NB];
  bf16x8 pf[NB], pn[NB];
  bf16x8 kf[8], vf[8];

#define KRD(i, so_) kf[i] = ldsb128(l3 + (so_) + (kbase ^ (((i) >> 1) << 6)) + ((i) & 1) * 4096)
#define VRD(db, so_) do { const s16x4 lo = vtr(l3 + (so_) + (vbase ^ ((db) << 5))); const s16x4 hi = vtr(l3 + (so_) + (vbase ^ ((db) << 5)) + 4096); \
      vf[db] = (bf16x8){lo[0], lo[1], lo[2], lo[3], hi[0], hi[1], hi[2], hi[3]}; } while (0)
#define QKI(i) do { _Pragma("unroll") for (int qb = 0; qb < NB; ++qb) s[(i) & 1][qb] = MFMA16(kf[i], qf[qb][(i) >> 1], ((i) >> 1) == 0 ? negm[qb] : s[(i) & 1][qb]); } while (0)
#define ROWMAX(qb) fmx(fmx(fmx(fmx(s[0][qb][0], s[0][qb][1]), s[0][qb][2]), fmx(fmx(s[0][qb][3], s[1][qb][0]), s[1][qb][1])), fmx(s[1][qb][2], s[1][qb][3]))
#define XLANE_MAX(bm) do { \
        { auto r = __builtin_amdgcn_permlane16_swap(__float_as_uint(bm), __float_as_uint(bm), false, false); bm = fmx(__uint_as_float(r[0]), __uint_as_float(r[1])); } \
        { auto r = __builtin_amdgcn_permlane32_swap(__float_as_uint(bm), __float_as_uint(bm), false, false); bm = fmx(__uint_as_float(r[0]), __uint_as_float(r[1])); } } while (0)
#define EXPP(pi) do { unsigned w_ = cvtpk(__builtin_amdgcn_exp2f(s[((pi) >> 1) & 1][(pi) >> 2][((pi) & 1) * 2]), __builtin_amdgcn_exp2f(s[((pi) >> 1) & 1][(pi) >> 2][((pi) & 1) * 2 + 1])); \
      PIN(w_); pw[(pi) >> 2][(pi) & 3] = w_; } while (0)
#define PVI(db) do { _Pragma("unroll") for (int qb = 0; qb < NB; ++qb) o[qb][db] = MFMA16(vf[db], pf[qb], o[qb][db]); } while (0)
#define LSUM() do { _Pragma("unroll") for (int qb = 0; qb < NB; ++qb) ls[qb] = MFMA16(ones, pf[qb], ls[qb]); } while (0)

  asm volatile("s_waitcnt vmcnt(8) lgkmcnt(0)\n\ts_barrier" ::: "memory");
  u32x4 pw[NB];
#pragma unroll
  for (int i = 0; i < 8; ++i) KRD(i, 0);
#pragma unroll
  for (int i = 0; i < 8; ++i) QKI(i);
#pragma unroll
  for (int qb = 0; qb < NB; ++qb) {
    float bm = ROWMAX(qb); XLANE_MAX(bm);
    negm[qb] -= bm; s[0][qb] -= bm; s[1][qb] -= bm;
  }
#pragma unroll
  for (int pi = 0; pi < 4 * NB; ++pi) EXPP(pi);
#pragma unroll
  for (int qb = 0; qb < NB; ++qb) pf[qb] = __builtin_bit_cast(bf16x8, pw[qb]);
  KRD(0, SLOT); KRD(1, SLOT);
  int sprev = 0, scur = 1;
  for (int t = 1; t < NTILE; ++t) {
    const int snext = (scur == NSLOT - 1) ? 0 : scur + 1;
    const int so = scur * SLOT, sp = sprev * SLOT;
    SBAR();
#pragma unroll
    for (int i = 0; i < 8; ++i) {
      if (i + 2 < 8) KRD(i + 2, so);
      else VRD(i - 6, sp);
      QKI(i);
      SBAR();
    }
    float mx[NB]; bool trig = false;
#pragma unroll
    for (int qb = 0; qb < NB; ++qb) { mx[qb] = ROWMAX(qb); trig |= (mx[qb] > THR); }
    float al[NB]; bool resc = false;
    if (__builtin_expect(__any(trig), 0)) {
      resc = true;
#pragma unroll
      for (int qb = 0; qb < NB; ++qb) {
        float bm = mx[qb]; XLANE_MAX(bm);
        const float d = fmx(bm, 0.f);
        negm[qb] -= d; s[0][qb] -= d; s[1][qb] -= d;
        al[qb] = __builtin_amdgcn_exp2f(-d);
      }
    }
    if (t + 2 < NTILE) asm volatile("s_waitcnt vmcnt(4)\n\ts_barrier" ::: "memory");
    else if (t + 1 < NTILE) asm volatile("s_waitcnt vmcnt(0)\n\ts_barrier" ::: "memory");
    const int ns = (sprev == 0) ? (NSLOT - 1) : (sprev - 1);
    const bool more = (t + 3 < NTILE);
    SBAR();
    LSUM();
#pragma unroll
    for (int db = 0; db < 8; ++db) {
      if (db + 2 < 8) VRD(db + 2, sp);
      else if (t + 1 < NTILE) KRD(db - 6, snext * SLOT);
      PVI(db);
      if (db < 4 && more) ISSUE1(t + 3, ns, db);
      if (NB == 3) { EXPP((3 * db) >> 1); if (db & 1) EXPP(((3 * db) >> 1) + 1); }
      else EXPP(db);
      SBAR();
    }
#pragma unroll
    for (int qb = 0; qb < NB; ++qb) pf[qb] = __builtin_bit_cast(bf16x8, pw[qb]);
    if (__builtin_expect(resc, 0)) {
#pragma unroll
      for (int qb = 0; qb < NB; ++qb) {
#pragma unroll
        for (int db = 0; db < 8; ++db) o[qb][db] *= al[qb];
        ls[qb] *= al[qb];
      }
    }
    sprev = scur; scur = snext;
  }
  LSUM();
#pragma unroll
  for (int db = 0; db < 8; ++db) { VRD(db, sprev * SLOT); PVI(db); }
#undef ISSUE
#pragma unroll
  for (int qb = 0; qb < NB; ++qb) {
    const float inv = 1.0f / ls[qb][0];
#pragma unroll
    for (int db = 0; db < 8; ++db) {
      u32x2 v = {cvtpk(o[qb][db][0] * inv, o[qb][db][1] * inv), cvtpk(o[qb][db][2] * inv, o[qb][db][3] * inv)};
      *(u32x2*)(Og + (size_t)((db >> 2) * SQ + (qb0 + qb) * 16 + i16) * 64 + (db & 3) * 16 + g * 4) = v;
    }
  }
}

__global__ __launch_bounds__(256, 2) void attn_kernel(const u16* __restrict__ xA, const u16* __restrict__ xB, const u16* __restrict__ wqA, const u16* __restrict__ wqB,
                                                      const u16* __restrict__ kA, const u16* __restrict__ vA,
                                                      const u16* __restrict__ kB, const u16* __restrict__ vB, u16* __restrict__ ows) {
  extern __shared__ __attribute__((aligned(16))) char lds[];
  const int item = blockIdx.x, blk = item >> 8, bh = item & 255, b = bh >> 5, h = bh & 31;
  const u16* K = (blk ? kB : kA) + (size_t)bh * SK * DH;
  const u16* V = (blk ? vB : vA) + (size_t)bh * SK * DH;
  const u16* X = (blk ? xB : xA) + (size_t)b * SQ * DM;
  const u16* W = (blk ? wqB : wqA) + h * DH;
  u16* O = ows + ((size_t)(blk * 8 + b) * SQ) * HD + (size_t)h * SQ * DH;
  const int tid = threadIdx.x, wid = __builtin_amdgcn_readfirstlane(tid >> 6);
  if (wid == 0) attn_wave<3, false>(X, W, K, V, O, lds, tid, wid, 0);
  else if (wid == 1) attn_wave<2, false>(X, W, K, V, O, lds, tid, wid, 3);
  else attn_wave<2, true>(X, W, K, V, O, lds, tid, wid, 1 + 2 * wid);
}

extern "C" void kernel_launch(void* const* d_in, const int* in_sizes, int n_in, void* d_out, int out_size, void* d_ws,
                              size_t ws_size, hipStream_t stream) {
  const u16* xA = (const u16*)d_in[0]; const u16* kA = (const u16*)d_in[1]; const u16* vA = (const u16*)d_in[2];
  const u16* wqA = (const u16*)d_in[3]; const u16* woA = (const u16*)d_in[4];
  const u16* xB = (const u16*)d_in[5]; const u16* kB = (const u16*)d_in[6]; const u16* vB = (const u16*)d_in[7];
  const u16* wqB = (const u16*)d_in[8]; const u16* woB = (const u16*)d_in[9];
  char* ws = (char*)d_ws;
  u16* qws = (u16*)ws;
  u16* ows = (u16*)(ws + (size_t)512 * SQ * DH * 2);
  float* part = (float*)(ws + (size_t)2 * 512 * SQ * DH * 2);
  hipFuncSetAttribute((const void*)attn_kernel, hipFuncAttributeMaxDynamicSharedMemorySize, NSLOT * SLOT);
  attn_kernel<<<512, 256, NSLOT * SLOT, stream>>>(xA, xB, wqA, wqB, kA, vA, kB, vB, ows);
  hipFuncSetAttribute((const void*)oproj_kernel, hipFuncAttributeMaxDynamicSharedMemorySize, OP_NSTG * OP_STAGE + 1024);
  oproj_kernel<<<240, 512, OP_NSTG * OP_STAGE + 1024, stream>>>(ows, woA, woB, part);
  oreduce_kernel<<<(16 * SQ * DM / 4 + 255) / 256, 256, 0, stream>>>(part, (u16*)d_out);
}
```

```cpp
#include <hip/hip_runtime.h>
#include <hip/hip_bf16.h>
#include <stdint.h>

constexpr int NB_ = 8, SQ = 144, DM = 640, NH = 32, SK = 1024, DH = 128;
constexpr int HD = NH * DH;
constexpr int MROWS = NB_ * SQ;
constexpr float QSCALE = 0.088388347648318447f * 1.4426950408889634f;

using bf16x8 = __attribute__((ext_vector_type(8))) short;
using s16x4 = __attribute__((ext_vector_type(4))) short;
using f32x4 = __attribute__((ext_vector_type(4))) float;
using u32x4 = __attribute__((ext_vector_type(4))) unsigned;
using u32x2 = __attribute__((ext_vector_type(2))) unsigned;
typedef short v4i16_t __attribute__((ext_vector_type(4)));
typedef __attribute__((address_space(3))) const char* lds_cptr;
typedef unsigned short u16;

#define SBAR() __builtin_amdgcn_sched_barrier(0)
#define PIN(x) asm volatile("" : "+v"(x))
#define MFMA16(a, b, c) __builtin_amdgcn_mfma_f32_16x16x32_bf16(a, b, c, 0, 0, 0)

typedef __bf16 bf16x2_t __attribute__((ext_vector_type(2)));
typedef float f32x2_t __attribute__((ext_vector_type(2)));
__device__ __forceinline__ unsigned cvtpk(float lo, float hi) {
  const f32x2_t v = {lo, hi};
  return __builtin_bit_cast(unsigned, __builtin_convertvector(v, bf16x2_t));
}
__device__ __forceinline__ s16x4 vtr(lds_cptr p) {
  return __builtin_bit_cast(s16x4, __builtin_amdgcn_ds_read_tr16_b64_v4i16((__attribute__((address_space(3))) v4i16_t*)p));
}
__device__ __forceinline__ bf16x8 ldsb128(lds_cptr p) {
  return *(const __attribute__((address_space(3))) bf16x8*)p;
}
__device__ __forceinline__ float fmx(float a, float b) { return __builtin_elementwise_maximum(a, b); }
__device__ __forceinline__ void glds16c(const void* sbase, unsigned voff, unsigned lds_base) {
  unsigned sv;
  asm volatile("s_mov_b32 %0, m0\n\ts_mov_b32 m0, %3\n\ts_nop 0\n\tglobal_load_lds_dwordx4 %1, %2\n\ts_mov_b32 m0, %0"
               : "=&s"(sv) : "v"(voff), "s"(sbase), "s"(lds_base) : "memory");
}
__device__ __forceinline__ void glds16(const void* sbase, unsigned voff, unsigned lds_base) {
  unsigned sv;
  asm volatile("s_mov_b32 %0, m0\n\ts_mov_b32 m0, %3\n\ts_nop 0\n\tglobal_load_lds_dwordx4 %1, %2 nt\n\ts_mov_b32 m0, %0"
               : "=&s"(sv) : "v"(voff), "s"(sbase), "s"(lds_base) : "memory");
}

#define OXL glds16c
#define OWL glds16c
#define OROT ((3 * (r / 5) + 4 * nt) % 21)
#define QROT 0
#define INRED 1
constexpr int OP_STAGE = 18432 + 16384, OP_NSTG = 4, OP_SPLIT = 3;
template <int MB, int NDMA>
__device__ __forceinline__ void oproj_wave(const u16* X, const u16* W, float* __restrict__ part, char* lds, int tid, int wid, int nst, int rot) {
  const int lane = tid & 63, g = lane >> 4, i16 = lane & 15;
  lds_cptr l3 = (lds_cptr)lds;
  const unsigned lds0 = (unsigned)(uintptr_t)lds;
  const int kk = wid & 1, ng = (wid >> 1) & 1, mb0 = (wid >> 2) * 5;
  const int xrow = wid * 8 + (lane >> 3), wrow = wid * 4 + g;
  const unsigned xoff = xrow * 128 + (((lane & 7) ^ (xrow & 7)) << 4);
  const unsigned woff = wrow * (DM * 2) + ((i16 ^ (((wrow & 3) | ((wrow >> 1) & 4)) << 1)) << 4);
#define OISSUE(s_) do { const unsigned sb_ = lds0 + ((s_) & 3) * OP_STAGE; const int sr_ = ((s_) + rot >= nst) ? (s_) + rot - nst : (s_) + rot; \
    const char* xs_ = (const char*)X + (size_t)sr_ * 18432; const char* ws_ = (const char*)W + (size_t)sr_ * (64 * DM * 2); \
    OXL(xs_, xoff, (unsigned)__builtin_amdgcn_readfirstlane(sb_ + wid * 1024)); \
    OXL(xs_ + 8192, xoff, (unsigned)__builtin_amdgcn_readfirstlane(sb_ + (wid + 8) * 1024)); \
    if (NDMA == 5) OXL(xs_ + 16384, xoff, (unsigned)__builtin_amdgcn_readfirstlane(sb_ + (wid + 16) * 1024)); \
    OWL(ws_, woff, (unsigned)__builtin_amdgcn_readfirstlane(sb_ + 18432 + wid * 1024)); \
    OWL(ws_ + 32 * DM * 2, woff, (unsigned)__builtin_amdgcn_readfirstlane(sb_ + 18432 + (wid + 8) * 1024)); } while (0)
  f32x4 acc[4][MB];
#pragma unroll
  for (int a = 0; a < 4; ++a)
#pragma unroll
    for (int b = 0; b < MB; ++b) acc[a][b] = f32x4{0.f, 0.f, 0.f, 0.f};
  OISSUE(0); OISSUE(1); OISSUE(2); OISSUE(3);
  const int xrd = (mb0 * 16 + i16) * 128 + (((4 * kk + g) ^ (i16 & 7)) << 4);
  const int wr = 8 * g + (i16 >> 2);
  const int wrd = 18432 + kk * 8192 + wr * 256 + ((((wr & 3) | ((wr >> 1) & 4))) << 5) + (lane & 3) * 8;
  bf16x8 wf[4], wn[4], xf[MB];
#define OWRD(dst, nb, so_) do { const s16x4 lo = vtr(l3 + (so_) + (wrd ^ ((4 * ng + (nb)) << 5))); const s16x4 hi = vtr(l3 + (so_) + (wrd ^ ((4 * ng + (nb)) << 5)) + 1024); \
      dst = (bf16x8){lo[0], lo[1], lo[2], lo[3], hi[0], hi[1], hi[2], hi[3]}; } while (0)
  asm volatile("s_waitcnt vmcnt(%0)\n\ts_barrier" :: "i"(3 * NDMA) : "memory");
#pragma unroll
  for (int nb = 0; nb < 4; ++nb) OWRD(wf[nb], nb, 0);
#pragma unroll
  for (int mb = 0; mb < MB; ++mb) xf[mb] = ldsb128(l3 + xrd + mb * 2048);
  for (int s = 0; s < nst; ++s) {
    if (s + 3 < nst) asm volatile("s_waitcnt vmcnt(%0) lgkmcnt(0)\n\ts_barrier" :: "i"(2 * NDMA) : "memory");
    else if (s + 2 < nst) asm volatile("s_waitcnt vmcnt(%0) lgkmcnt(0)\n\ts_barrier" :: "i"(NDMA) : "memory");
    else asm volatile("s_waitcnt vmcnt(0) lgkmcnt(0)\n\ts_barrier" ::: "memory");
    if (s + 4 < nst) OISSUE(s + 4);
    const int sn = (s + 1 < nst) ? ((s + 1) & 3) * OP_STAGE : (s & 3) * OP_STAGE;
    SBAR();
#pragma unroll
    for (int mb = 0; mb < MB; ++mb) {
      if (mb < 4) OWRD(wn[mb], mb, sn);
#pragma unroll
      for (int nb = 0; nb < 4; ++nb) acc[nb][mb] = MFMA16(wf[nb], xf[mb], acc[nb][mb]);
      xf[mb] = ldsb128(l3 + sn + xrd + mb * 2048);
      SBAR();
    }
#pragma unroll
    for (int nb = 0; nb < 4; ++nb) wf[nb] = wn[nb];
  }
#undef OISSUE
  asm volatile("s_waitcnt lgkmcnt(0)\n\ts_barrier" ::: "memory");
  char* red = lds + (wid >> 1) * 20480 + lane * 16;
  if (kk == 1) {
#pragma unroll
    for (int mb = 0; mb < MB; ++mb)
#pragma unroll
      for (int nb = 0; nb < 4; ++nb) *(f32x4*)(red + (mb * 4 + nb) * 1024) = acc[nb][mb];
  }
  asm volatile("s_waitcnt lgkmcnt(0)\n\ts_barrier" ::: "memory");
  if (kk == 0) {
#pragma unroll
    for (int mb = 0; mb < MB; ++mb)
#pragma unroll
      for (int nb = 0; nb < 4; ++nb) {
        const f32x4 v = acc[nb][mb] + *(const f32x4*)(red + (mb * 4 + nb) * 1024);
        float* dst = part + (size_t)((mb0 + mb) * 16 + i16) * DM + (4 * ng + nb) * 16 + g * 4;
        asm volatile("global_store_dwordx4 %0, %1, off sc1\n\ts_nop 1" :: "v"(dst), "v"(v) : "memory");
      }
  }
}

__global__ __launch_bounds__(512, 2) void oproj_kernel(const u16* __restrict__ ows, const u16* __restrict__ woA,
                                                       const u16* __restrict__ woB, float* part, unsigned* cnt, u16* __restrict__ out) {
  extern __shared__ __attribute__((aligned(16))) char lds[];
  const int L = (blockIdx.x & 7) * 30 + (blockIdx.x >> 3), grp = L / 40, r = L % 40, blk = grp / 3, ksp = grp % 3, nt = r % 5, bb = blk * 8 + r / 5;
  const int s0 = ksp == 0 ? 0 : (ksp == 1 ? 22 : 43), nst = ksp == 0 ? 22 : 21;
  const u16* o = ows + (size_t)bb * SQ * HD + (size_t)s0 * SQ * 64;
  const u16* wo = (blk ? woB : woA) + (size_t)s0 * 64 * DM + nt * 128;
  float* p = part + ((size_t)ksp * 16 + bb) * SQ * DM + nt * 128;
  const int tid = threadIdx.x, wid = __builtin_amdgcn_readfirstlane(tid >> 6);
  const int rot = OROT;
  if (wid < 2) oproj_wave<5, 5>(o, wo, p, lds, tid, wid, nst, rot);
  else if (wid < 4) oproj_wave<5, 4>(o, wo, p, lds, tid, wid, nst, rot);
  else oproj_wave<4, 4>(o, wo, p, lds, tid, wid, nst, rot);
  asm volatile("s_waitcnt vmcnt(0)" ::: "memory");
  __syncthreads();
  if (tid == 0) {
    unsigned* c = cnt + (bb * 5 + nt) * 16;
    __hip_atomic_fetch_add(c, 1u, __ATOMIC_RELAXED, __HIP_MEMORY_SCOPE_AGENT);
    for (int spin = 0; spin < (1 << 22); ++spin) {
      if (__hip_atomic_load(c, __ATOMIC_RELAXED, __HIP_MEMORY_SCOPE_AGENT) >= (unsigned)OP_SPLIT) break;
      __builtin_amdgcn_s_sleep(2);
    }
    __builtin_amdgcn_fence(__ATOMIC_ACQUIRE, "agent");
    asm volatile("s_waitcnt vmcnt(0)" ::: "memory");
  }
  __syncthreads();
  {
    const float* p0 = part + (size_t)bb * SQ * DM + nt * 128;
#pragma unroll
    for (int it = 0; it < 3; ++it) {
      const int u = it * 512 + tid, row = ksp * 48 + (u >> 5), c4 = (u & 31) * 4;
      f32x4 sum = *(const f32x4*)(p0 + (size_t)row * DM + c4);
#pragma unroll
      for (int k = 1; k < OP_SPLIT; ++k) sum += *(const f32x4*)(p0 + (size_t)k * 16 * SQ * DM + (size_t)row * DM + c4);
      u32x2 v = {cvtpk(sum[0], sum[1]), cvtpk(sum[2], sum[3])};
      *(u32x2*)(out + ((size_t)((bb & 7) * SQ + row) * 2 + blk) * DM + nt * 128 + c4) = v;
    }
  }
}

__global__ __launch_bounds__(256) void oreduce_kernel(const float* __restrict__ part, u16* __restrict__ out) {
  const int idx = blockIdx.x * 256 + threadIdx.x;
  const int total = 16 * SQ * DM / 4;
  if (idx >= total) return;
  f32x4 s = *(const f32x4*)(part + (size_t)idx * 4);
#pragma unroll
  for (int k = 1; k < OP_SPLIT; ++k) s += *(const f32x4*)(part + (size_t)k * 16 * SQ * DM + (size_t)idx * 4);
  const int e = idx * 4, n = e % DM, row = (e / DM) % SQ, bb = e / (DM * SQ), blk = bb >> 3, b = bb & 7;
  u32x2 v = {cvtpk(s[0], s[1]), cvtpk(s[2], s[3])};
  *(u32x2*)(out + ((size_t)(b * SQ + row) * 2 + blk) * DM + n) = v;
}

constexpr int SLOT = 16384, NSLOT = 5, NTILE = SK / 32;
constexpr float THR = 6.0f;
#define ROLE_ROT 0
#define ROTF 0
#define WLOAD glds16c
#define XLOAD(p) (*(p))
#define KVAT -1
#define AMAP 0
#define DELAY 0
#define FUSED 1
#define XDIST 4
#define SCHED 1

template <int NB, bool X5, int NP, int P0>
__device__ __forceinline__ void attn_wave(const u16* Xg, const u16* Wg, const u16* Kg, const u16* Vg, u16* __restrict__ Og,
                                          char* lds, int tid, int wid, int qb0) {
  const int lane = tid & 63, g = lane >> 4, i16 = lane & 15;
  lds_cptr l3 = (lds_cptr)lds;
  const unsigned lds0 = (unsigned)(uintptr_t)lds;
  const unsigned kvk = g * 256 + ((i16 ^ g) << 4), kvv = g * 256 + ((i16 ^ (g << 1)) << 4);
#define ISSUE1(t, slot, i) do { const int p_ = P0 + (i); \
    if (p_ < 8) glds16((const char*)Kg + (size_t)(t) * 8192 + p_ * 1024, kvk ^ ((p_ & 3) << 6), (unsigned)__builtin_amdgcn_readfirstlane(lds0 + (slot) * SLOT + p_ * 1024)); \
    else glds16((const char*)Vg + (size_t)(t) * 8192 + (p_ - 8) * 1024, kvv ^ ((p_ & 1) << 7), (unsigned)__builtin_amdgcn_readfirstlane(lds0 + (slot) * SLOT + p_ * 1024)); } while (0)
#define ISSUE(t, slot) do { if (NP > 0) ISSUE1(t, slot, 0); if (NP > 1) ISSUE1(t, slot, 1); if (NP > 2) ISSUE1(t, slot, 2); if (NP > 3) ISSUE1(t, slot, 3); if (NP > 4) ISSUE1(t, slot, 4); if (NP > 5) ISSUE1(t, slot, 5); } while (0)
  bf16x8 qf[NB][4];
  {
    constexpr int NST = DM / 32, XB = 16384, XSZ = 18432, WB = 53248;
    const int xrow = wid * 8 + (lane >> 3);
    const unsigned xoff = xrow * (DM * 2) + (((lane & 7) ^ (xrow & 7)) << 4);
    unsigned woff[2];
#pragma unroll
    for (int it = 0; it < 2; ++it) {
      const int row = wid * 8 + it * 4 + g;
      woff[it] = row * (HD * 2) + ((i16 ^ (((row & 3) | ((row >> 1) & 4)) << 1)) << 4);
    }
    constexpr bool x5 = X5;
    const int qrot = QROT;
#define RS(s_) (((s_) + qrot >= NST / 2) ? (s_) + qrot - NST / 2 : (s_) + qrot)
#define WISSUE(c) do { const char* ws_ = (const char*)Wg + (size_t)(RS((c) >> 1) * 2 + ((c) & 1)) * (32 * HD * 2); _Pragma("unroll") for (int it = 0; it < 2; ++it) \
      glds16c(ws_, woff[it], (unsigned)__builtin_amdgcn_readfirstlane(lds0 + WB + ((c) % 3) * 8192 + wid * 2048 + it * 1024)); } while (0)
#define XISSUE(s_) do { const char* xs_ = (const char*)Xg + RS(s_) * 128; _Pragma("unroll") for (int it = 0; it < 4; ++it) \
      glds16c(xs_ + it * 32 * DM * 2, xoff, (unsigned)__builtin_amdgcn_readfirstlane(lds0 + XB + ((s_) & 1) * XSZ + (wid + 4 * it) * 1024)); \
      if (x5) glds16c(xs_ + (128 - 16) * DM * 2, xoff, (unsigned)__builtin_amdgcn_readfirstlane(lds0 + XB + ((s_) & 1) * XSZ + (wid + 14) * 1024)); } while (0)
#define VMW(n4, n5, tail) do { if (x5) asm volatile("s_waitcnt vmcnt(%0)" tail :: "i"(n5) : "memory"); else asm volatile("s_waitcnt vmcnt(%0)" tail :: "i"(n4) : "memory"); } while (0)
    f32x4 acc[NB][8];
#pragma unroll
    for (int qb = 0; qb < NB; ++qb)
#pragma unroll
      for (int db = 0; db < 8; ++db) acc[qb][db] = f32x4{0.f, 0.f, 0.f, 0.f};
    XISSUE(0); WISSUE(0); WISSUE(1); XISSUE(1); WISSUE(2);
    ISSUE(0, 0);
    const int wr = 8 * g + (i16 >> 2);
    const int wrd = WB + wr * 256 + ((((wr & 3) | ((wr >> 1) & 4))) << 5) + (lane & 3) * 8;
    const int xrd = XB + (qb0 * 16 + i16) * 128 + ((g ^ (i16 & 7)) << 4);
    bf16x8 wf[8], xa[NB], xb_[NB];
#define WRD(db, c_) do { const s16x4 lo = vtr(l3 + ((c_) % 3) * 8192 + (wrd ^ ((db) << 5))); const s16x4 hi = vtr(l3 + ((c_) % 3) * 8192 + (wrd ^ ((db) << 5)) + 1024); \
        wf[db] = (bf16x8){lo[0], lo[1], lo[2], lo[3], hi[0], hi[1], hi[2], hi[3]}; } while (0)
#define XRD(dst, c_) do { _Pragma("unroll") for (int qb = 0; qb < NB; ++qb) dst[qb] = ldsb128(l3 + (((c_) >> 1) & 1) * XSZ + (xrd ^ (((c_) & 1) << 6)) + qb * 2048); } while (0)
    asm volatile("s_waitcnt vmcnt(%0)\n\ts_barrier" :: "i"((X5 ? 5 : 4) + 2 + NP) : "memory");
#pragma unroll
    for (int db = 0; db < 8; ++db) WRD(db, 0);
    XRD(xa, 0);
#pragma unroll
    for (int c = 0; c < NST; ++c) {
      if (c == 0) asm volatile("s_waitcnt lgkmcnt(0)\n\ts_barrier" ::: "memory");
      else if (c == 1) asm volatile("s_waitcnt vmcnt(%0) lgkmcnt(0)\n\ts_barrier" :: "i"(2 + NP) : "memory");
      else if (c + 1 < NST) {
        if (c & 1) asm volatile("s_waitcnt vmcnt(%0) lgkmcnt(0)\n\ts_barrier" :: "i"((c + 2 < NST) ? 2 : 0) : "memory");
        else VMW(((c + 2 < NST) ? 2 : 0) + ((((c - 1) >> 1) + 2 < NST / 2) ? 4 : 0), ((c + 2 < NST) ? 2 : 0) + ((((c - 1) >> 1) + 2 < NST / 2) ? 5 : 0), " lgkmcnt(0)\n\ts_barrier");
      }
      if (c + 3 < NST) WISSUE(c + 3);
      if ((c & 1) && (c >> 1) + 2 < NST / 2) XISSUE((c >> 1) + 2);
      if (c + 1 < NST) { if (c & 1) XRD(xa, c + 1); else XRD(xb_, c + 1); }
      SBAR();
#pragma unroll
      for (int db = 0; db < 8; ++db) {
#pragma unroll
        for (int qb = 0; qb < NB; ++qb) acc[qb][db] = MFMA16(wf[db], (c & 1) ? xb_[qb] : xa[qb], acc[qb][db]);
        if (c + 1 < NST) WRD(db, c + 1);
        SBAR();
      }
    }
#undef WRD
#undef XRD
#undef WISSUE
#undef XISSUE
    asm volatile("s_waitcnt lgkmcnt(0)\n\ts_barrier" ::: "memory");
    ISSUE(1, 1); ISSUE(2, 2); ISSUE(3, 3);
    char* scr = lds + 4 * SLOT + wid * 4096;
#pragma unroll
    for (int qb = 0; qb < NB; ++qb) {
#pragma unroll
      for (int db = 0; db < 8; ++db) {
        u32x2 v = {cvtpk(acc[qb][db][0] * QSCALE, acc[qb][db][1] * QSCALE), cvtpk(acc[qb][db][2] * QSCALE, acc[qb][db][3] * QSCALE)};
        *(u32x2*)(scr + i16 * 256 + (((db * 2 + (g >> 1)) ^ i16) << 4) + (g & 1) * 8) = v;
      }
      asm volatile("s_waitcnt lgkmcnt(0)" ::: "memory");
#pragma unroll
      for (int ks = 0; ks < 4; ++ks) qf[qb][ks] = *(const bf16x8*)(scr + i16 * 256 + (((ks * 4 + g) ^ i16) << 4));
      asm volatile("s_waitcnt lgkmcnt(0)" ::: "memory");
#pragma unroll
      for (int ks = 0; ks < 4; ++ks) PIN(qf[qb][ks]);
    }
  }

  f32x4 o[NB][8], ls[NB], negm[NB];
#pragma unroll
  for (int qb = 0; qb < NB; ++qb) {
#pragma unroll
    for (int db = 0; db < 8; ++db) o[qb][db] = f32x4{0.f, 0.f, 0.f, 0.f};
    ls[qb] = f32x4{0.f, 0.f, 0.f, 0.f};
    negm[qb] = f32x4{0.f, 0.f, 0.f, 0.f};
  }
  const bf16x8 ones = {0x3F80, 0x3F80, 0x3F80, 0x3F80, 0x3F80, 0x3F80, 0x3F80, 0x3F80};
  const int kbase = i16 * 256 + ((g ^ i16) << 4);
  const int vrow = 4 * g + (i16 >> 2);
  const int vbase = 8192 + vrow * 256 + ((vrow & 7) << 5) + (lane & 3) * 8;
  f32x4 s[2][NB];
  bf16x8 pf[NB], pn[NB];
  bf16x8 kf[8], vf[8];

#define KRD(i, so_) kf[i] = ldsb128(l3 + (so_) + (kbase ^ (((i) >> 1) << 6)) + ((i) & 1) * 4096)
#define VRD(db, so_) do { const s16x4 lo = vtr(l3 + (so_) + (vbase ^ ((db) << 5))); const s16x4 hi = vtr(l3 + (so_) + (vbase ^ ((db) << 5)) + 4096); \
      vf[db] = (bf16x8){lo[0], lo[1], lo[2], lo[3], hi[0], hi[1], hi[2], hi[3]}; } while (0)
#define QKI(i) do { _Pragma("unroll") for (int qb = 0; qb < NB; ++qb) s[(i) & 1][qb] = MFMA16(kf[i], qf[qb][(i) >> 1], ((i) >> 1) == 0 ? negm[qb] : s[(i) & 1][qb]); } while (0)
#define ROWMAX(qb) fmx(fmx(fmx(fmx(s[0][qb][0], s[0][qb][1]), s[0][qb][2]), fmx(fmx(s[0][qb][3], s[1][qb][0]), s[1][qb][1])), fmx(s[1][qb][2], s[1][qb][3]))
#define XLANE_MAX(bm) do { \
        { auto r = __builtin_amdgcn_permlane16_swap(__float_as_uint(bm), __float_as_uint(bm), false, false); bm = fmx(__uint_as_float(r[0]), __uint_as_float(r[1])); } \
        { auto r = __builtin_amdgcn_permlane32_swap(__float_as_uint(bm), __float_as_uint(bm), false, false); bm = fmx(__uint_as_float(r[0]), __uint_as_float(r[1])); } } while (0)
#define EXPP(pi) do { unsigned w_ = cvtpk(__builtin_amdgcn_exp2f(s[((pi) >> 1) & 1][(pi) >> 2][((pi) & 1) * 2]), __builtin_amdgcn_exp2f(s[((pi) >> 1) & 1][(pi) >> 2][((pi) & 1) * 2 + 1])); \
      PIN(w_); pw[(pi) >> 2][(pi) & 3] = w_; } while (0)
#define PVI(db) do { _Pragma("unroll") for (int qb = 0; qb < NB; ++qb) o[qb][db] = MFMA16(vf[db], pf[qb], o[qb][db]); } while (0)
#define LSUM() do { _Pragma("unroll") for (int qb = 0; qb < NB; ++qb) ls[qb] = MFMA16(ones, pf[qb], ls[qb]); } while (0)

  asm volatile("s_waitcnt vmcnt(%0) lgkmcnt(0)\n\ts_barrier" :: "i"(2 * NP) : "memory");
  u32x4 pw[NB];
#pragma unroll
  for (int i = 0; i < 8; ++i) KRD(i, 0);
#pragma unroll
  for (int i = 0; i < 8; ++i) QKI(i);
#pragma unroll
  for (int qb = 0; qb < NB; ++qb) {
    float bm = ROWMAX(qb); XLANE_MAX(bm);
    negm[qb] -= bm; s[0][qb] -= bm; s[1][qb] -= bm;
  }
#pragma unroll
  for (int pi = 0; pi < 4 * NB; ++pi) EXPP(pi);
#pragma unroll
  for (int qb = 0; qb < NB; ++qb) pf[qb] = __builtin_bit_cast(bf16x8, pw[qb]);
  KRD(0, SLOT); KRD(1, SLOT);
  int sprev = 0, scur = 1;
  for (int t = 1; t < NTILE; ++t) {
    const int snext = (scur == NSLOT - 1) ? 0 : scur + 1;
    const int so = scur * SLOT, sp = sprev * SLOT;
    SBAR();
#pragma unroll
    for (int i = 0; i < 8; ++i) {
      if (i + 2 < 8) KRD(i + 2, so);
      else VRD(i - 6, sp);
      QKI(i);
      SBAR();
    }
    float mx[NB]; bool trig = false;
#pragma unroll
    for (int qb = 0; qb < NB; ++qb) { mx[qb] = ROWMAX(qb); trig |= (mx[qb] > THR); }
    float al[NB]; bool resc = false;
    if (__builtin_expect(__any(trig), 0)) {
      resc = true;
#pragma unroll
      for (int qb = 0; qb < NB; ++qb) {
        float bm = mx[qb]; XLANE_MAX(bm);
        const float d = fmx(bm, 0.f);
        negm[qb] -= d; s[0][qb] -= d; s[1][qb] -= d;
        al[qb] = __builtin_amdgcn_exp2f(-d);
      }
    }
    if (t + 2 < NTILE) asm volatile("s_waitcnt vmcnt(%0)\n\ts_barrier" :: "i"(NP) : "memory");
    else if (t + 1 < NTILE) asm volatile("s_waitcnt vmcnt(0)\n\ts_barrier" ::: "memory");
    const int ns = (sprev == 0) ? (NSLOT - 1) : (sprev - 1);
    const bool more = (t + 3 < NTILE);
    SBAR();
    LSUM();
#pragma unroll
    for (int db = 0; db < 8; ++db) {
      if (db + 2 < 8) VRD(db + 2, sp);
      else if (t + 1 < NTILE) KRD(db - 6, snext * SLOT);
      PVI(db);
      if (db < NP && more) ISSUE1(t + 3, ns, (db < NP ? db : 0));
      if (NB == 3) { EXPP((3 * db) >> 1); if (db & 1) EXPP(((3 * db) >> 1) + 1); }
      else EXPP(db);
      SBAR();
    }
#pragma unroll
    for (int qb = 0; qb < NB; ++qb) pf[qb] = __builtin_bit_cast(bf16x8, pw[qb]);
    if (__builtin_expect(resc, 0)) {
#pragma unroll
      for (int qb = 0; qb < NB; ++qb) {
#pragma unroll
        for (int db = 0; db < 8; ++db) o[qb][db] *= al[qb];
        ls[qb] *= al[qb];
      }
    }
    sprev = scur; scur = snext;
  }
  LSUM();
#pragma unroll
  for (int db = 0; db < 8; ++db) { VRD(db, sprev * SLOT); PVI(db); }
#undef ISSUE
#pragma unroll
  for (int qb = 0; qb < NB; ++qb) {
    const float inv = 1.0f / ls[qb][0];
#pragma unroll
    for (int db = 0; db < 8; ++db) {
      u32x2 v = {cvtpk(o[qb][db][0] * inv, o[qb][db][1] * inv), cvtpk(o[qb][db][2] * inv, o[qb][db][3] * inv)};
      *(u32x2*)(Og + (size_t)((db >> 2) * SQ + (qb0 + qb) * 16 + i16) * 64 + (db & 3) * 16 + g * 4) = v;
    }
  }
}

__global__ __launch_bounds__(256, 2) void attn_kernel(const u16* __restrict__ xA, const u16* __restrict__ xB, const u16* __restrict__ wqA, const u16* __restrict__ wqB,
                                                      const u16* __restrict__ kA, const u16* __restrict__ vA,
                                                      const u16* __restrict__ kB, const u16* __restrict__ vB, u16* __restrict__ ows, unsigned* __restrict__ cnt) {
  extern __shared__ __attribute__((aligned(16))) char lds[];
  if (blockIdx.x < 80 && threadIdx.x == 0) cnt[blockIdx.x * 16] = 0u;
  const int item = blockIdx.x, blk = item >> 8, bh = item & 255, b = bh >> 5, h = bh & 31;
  const u16* K = (blk ? kB : kA) + (size_t)bh * SK * DH;
  const u16* V = (blk ? vB : vA) + (size_t)bh * SK * DH;
  const u16* X = (blk ? xB : xA) + (size_t)b * SQ * DM;
  const u16* W = (blk ? wqB : wqA) + h * DH;
  u16* O = ows + ((size_t)(blk * 8 + b) * SQ) * HD + (size_t)h * SQ * DH;
  const int tid = threadIdx.x, wid = __builtin_amdgcn_readfirstlane(tid >> 6);
  if (wid == 0) attn_wave<3, false, 0, 0>(X, W, K, V, O, lds, tid, wid, 0);
  else if (wid == 1) attn_wave<2, false, 6, 0>(X, W, K, V, O, lds, tid, wid, 3);
  else if (wid == 2) attn_wave<2, true, 5, 6>(X, W, K, V, O, lds, tid, wid, 5);
  else attn_wave<2, true, 5, 11>(X, W, K, V, O, lds, tid, wid, 7);
}

extern "C" void kernel_launch(void* const* d_in, const int* in_sizes, int n_in, void* d_out, int out_size, void* d_ws,
                              size_t ws_size, hipStream_t stream) {
  const u16* xA = (const u16*)d_in[0]; const u16* kA = (const u16*)d_in[1]; const u16* vA = (const u16*)d_in[2];
  const u16* wqA = (const u16*)d_in[3]; const u16* woA = (const u16*)d_in[4];
  const u16* xB = (const u16*)d_in[5]; const u16* kB = (const u16*)d_in[6]; const u16* vB = (const u16*)d_in[7];
  const u16* wqB = (const u16*)d_in[8]; const u16* woB = (const u16*)d_in[9];
  char* ws = (char*)d_ws;
  unsigned* cnt = (unsigned*)ws;
  u16* ows = (u16*)(ws + 8192);
  float* part = (float*)(ws + 8192 + (size_t)16 * SQ * HD * 2);
  (void)hipFuncSetAttribute((const void*)attn_kernel, hipFuncAttributeMaxDynamicSharedMemorySize, NSLOT * SLOT);
  (void)hipFuncSetAttribute((const void*)oproj_kernel, hipFuncAttributeMaxDynamicSharedMemorySize, OP_NSTG * OP_STAGE);
  attn_kernel<<<512, 256, NSLOT * SLOT, stream>>>(xA, xB, wqA, wqB, kA, vA, kB, vB, ows, cnt);
  oproj_kernel<<<240, 512, OP_NSTG * OP_STAGE, stream>>>(ows, woA, woB, part, cnt, (u16*)d_out);
}
```

```cpp
#include <hip/hip_runtime.h>
#include <hip/hip_bf16.h>
#include <stdint.h>

constexpr int NB_ = 8, SQ = 144, DM = 640, NH = 32, SK = 1024, DH = 128;
constexpr int HD = NH * DH;
constexpr int MROWS = NB_ * SQ;
constexpr float QSCALE = 0.088388347648318447f * 1.4426950408889634f;

using bf16x8 = __attribute__((ext_vector_type(8))) short;
using s16x4 = __attribute__((ext_vector_type(4))) short;
using f32x4 = __attribute__((ext_vector_type(4))) float;
using u32x4 = __attribute__((ext_vector_type(4))) unsigned;
using u32x2 = __attribute__((ext_vector_type(2))) unsigned;
typedef short v4i16_t __attribute__((ext_vector_type(4)));
typedef __attribute__((address_space(3))) const char* lds_cptr;
typedef unsigned short u16;

#define SBAR() __builtin_amdgcn_sched_barrier(0)
#define PIN(x) asm volatile("" : "+v"(x))
#define MFMA16(a, b, c) __builtin_amdgcn_mfma_f32_16x16x32_bf16(a, b, c, 0, 0, 0)

typedef __bf16 bf16x2_t __attribute__((ext_vector_type(2)));
typedef float f32x2_t __attribute__((ext_vector_type(2)));
__device__ __forceinline__ unsigned cvtpk(float lo, float hi) {
  const f32x2_t v = {lo, hi};
  return __builtin_bit_cast(unsigned, __builtin_convertvector(v, bf16x2_t));
}
__device__ __forceinline__ s16x4 vtr(lds_cptr p) {
  return __builtin_bit_cast(s16x4, __builtin_amdgcn_ds_read_tr16_b64_v4i16((__attribute__((address_space(3))) v4i16_t*)p));
}
__device__ __forceinline__ bf16x8 ldsb128(lds_cptr p) {
  return *(const __attribute__((address_space(3))) bf16x8*)p;
}
__device__ __forceinline__ float fmx(float a, float b) { return __builtin_elementwise_maximum(a, b); }
__device__ __forceinline__ void glds16c(const void* sbase, unsigned voff, unsigned lds_base) {
  unsigned sv;
  asm volatile("s_mov_b32 %0, m0\n\ts_mov_b32 m0, %3\n\ts_nop 0\n\tglobal_load_lds_dwordx4 %1, %2\n\ts_mov_b32 m0, %0"
               : "=&s"(sv) : "v"(voff), "s"(sbase), "s"(lds_base) : "memory");
}
__device__ __forceinline__ void glds16(const void* sbase, unsigned voff, unsigned lds_base) {
  unsigned sv;
  asm volatile("s_mov_b32 %0, m0\n\ts_mov_b32 m0, %3\n\ts_nop 0\n\tglobal_load_lds_dwordx4 %1, %2 nt\n\ts_mov_b32 m0, %0"
               : "=&s"(sv) : "v"(voff), "s"(sbase), "s"(lds_base) : "memory");
}

constexpr int OP_STAGE = 18432 + 16384, OP_NSTG = 4, OP_SPLIT = 3;
template <int MB, int NDMA>
__device__ __forceinline__ void oproj_wave(const u16* X, const u16* W, float* __restrict__ part, char* lds, int tid, int wid, int nst, int rot) {
  const int lane = tid & 63, g = lane >> 4, i16 = lane & 15;
  lds_cptr l3 = (lds_cptr)lds;
  const unsigned lds0 = (unsigned)(uintptr_t)lds;
  const int kk = wid & 1, ng = (wid >> 1) & 1, mb0 = (wid >> 2) * 5;
  const int xrow = wid * 8 + (lane >> 3), wrow = wid * 4 + g;
  const unsigned xoff = xrow * 128 + (((lane & 7) ^ (xrow & 7)) << 4);
  const unsigned woff = wrow * (DM * 2) + ((i16 ^ (((wrow & 3) | ((wrow >> 1) & 4)) << 1)) << 4);
#define OISSUE1(s_, i_) do { const unsigned sb_ = lds0 + ((s_) & 3) * OP_STAGE; const int sr_ = ((s_) + rot >= nst) ? (s_) + rot - nst : (s_) + rot; \
    const char* xs_ = (const char*)X + (size_t)sr_ * 18432; const char* ws_ = (const char*)W + (size_t)sr_ * (64 * DM * 2); \
    if ((i_) == 0) glds16c(xs_, xoff, (unsigned)__builtin_amdgcn_readfirstlane(sb_ + wid * 1024)); \
    if ((i_) == 1) glds16c(xs_ + 8192, xoff, (unsigned)__builtin_amdgcn_readfirstlane(sb_ + (wid + 8) * 1024)); \
    if ((i_) == 2) glds16c(ws_, woff, (unsigned)__builtin_amdgcn_readfirstlane(sb_ + 18432 + wid * 1024)); \
    if ((i_) == 3) glds16c(ws_ + 32 * DM * 2, woff, (unsigned)__builtin_amdgcn_readfirstlane(sb_ + 18432 + (wid + 8) * 1024)); \
    if ((i_) == 4 && NDMA == 5) glds16c(xs_ + 16384, xoff, (unsigned)__builtin_amdgcn_readfirstlane(sb_ + (wid + 16) * 1024)); } while (0)
#define OISSUE(s_) do { OISSUE1(s_, 0); OISSUE1(s_, 1); OISSUE1(s_, 2); OISSUE1(s_, 3); OISSUE1(s_, 4); } while (0)
  f32x4 acc[4][MB];
#pragma unroll
  for (int a = 0; a < 4; ++a)
#pragma unroll
    for (int b = 0; b < MB; ++b) acc[a][b] = f32x4{0.f, 0.f, 0.f, 0.f};
  OISSUE(0); OISSUE(1); OISSUE(2); OISSUE(3);
  const int xrd = (mb0 * 16 + i16) * 128 + (((4 * kk + g) ^ (i16 & 7)) << 4);
  const int wr = 8 * g + (i16 >> 2);
  const int wrd = 18432 + kk * 8192 + wr * 256 + ((((wr & 3) | ((wr >> 1) & 4))) << 5) + (lane & 3) * 8;
  bf16x8 wf[4], wn[4], xf[MB];
#define OWRD(dst, nb, so_) do { const s16x4 lo = vtr(l3 + (so_) + (wrd ^ ((4 * ng + (nb)) << 5))); const s16x4 hi = vtr(l3 + (so_) + (wrd ^ ((4 * ng + (nb)) << 5)) + 1024); \
      dst = (bf16x8){lo[0], lo[1], lo[2], lo[3], hi[0], hi[1], hi[2], hi[3]}; } while (0)
  asm volatile("s_waitcnt vmcnt(%0)\n\ts_barrier" :: "i"(3 * NDMA) : "memory");
#pragma unroll
  for (int nb = 0; nb < 4; ++nb) OWRD(wf[nb], nb, 0);
#pragma unroll
  for (int mb = 0; mb < MB; ++mb) xf[mb] = ldsb128(l3 + xrd + mb * 2048);
  for (int s = 0; s < nst; ++s) {
    if (s + 3 < nst) asm volatile("s_waitcnt vmcnt(%0) lgkmcnt(0)\n\ts_barrier" :: "i"(2 * NDMA) : "memory");
    else if (s + 2 < nst) asm volatile("s_waitcnt vmcnt(%0) lgkmcnt(0)\n\ts_barrier" :: "i"(NDMA) : "memory");
    else asm volatile("s_waitcnt vmcnt(0) lgkmcnt(0)\n\ts_barrier" ::: "memory");
    const bool more = (s + 4 < nst);
    const int sn = (s + 1 < nst) ? ((s + 1) & 3) * OP_STAGE : (s & 3) * OP_STAGE;
    SBAR();
#pragma unroll
    for (int mb = 0; mb < MB; ++mb) {
      if (mb < 4) OWRD(wn[mb], mb, sn);
#pragma unroll
      for (int nb = 0; nb < 4; ++nb) acc[nb][mb] = MFMA16(wf[nb], xf[mb], acc[nb][mb]);
      xf[mb] = ldsb128(l3 + sn + xrd + mb * 2048);
      if (more) OISSUE1(s + 4, mb);
      SBAR();
    }
#pragma unroll
    for (int nb = 0; nb < 4; ++nb) wf[nb] = wn[nb];
  }
#undef OISSUE
#undef OISSUE1
  asm volatile("s_waitcnt lgkmcnt(0)\n\ts_barrier" ::: "memory");
  char* red = lds + (wid >> 1) * 20480 + lane * 16;
  if (kk == 1) {
#pragma unroll
    for (int mb = 0; mb < MB; ++mb)
#pragma unroll
      for (int nb = 0; nb < 4; ++nb) *(f32x4*)(red + (mb * 4 + nb) * 1024) = acc[nb][mb];
  }
  asm volatile("s_waitcnt lgkmcnt(0)\n\ts_barrier" ::: "memory");
  if (kk == 0) {
#pragma unroll
    for (int mb = 0; mb < MB; ++mb)
#pragma unroll
      for (int nb = 0; nb < 4; ++nb) {
        const f32x4 v = acc[nb][mb] + *(const f32x4*)(red + (mb * 4 + nb) * 1024);
        float* dst = part + (size_t)((mb0 + mb) * 16 + i16) * DM + (4 * ng + nb) * 16 + g * 4;
        asm volatile("global_store_dwordx4 %0, %1, off sc1\n\ts_nop 1" :: "v"(dst), "v"(v) : "memory");
      }
  }
}

__global__ __launch_bounds__(512, 2) void oproj_kernel(const u16* __restrict__ ows, const u16* __restrict__ woA,
                                                       const u16* __restrict__ woB, float* part, unsigned* cnt, u16* __restrict__ out) {
  extern __shared__ __attribute__((aligned(16))) char lds[];
  const int L = (blockIdx.x & 7) * 30 + (blockIdx.x >> 3), grp = L / 40, r = L % 40, blk = grp / 3, ksp = grp % 3, nt = r % 5, bb = blk * 8 + r / 5;
  const int s0 = ksp == 0 ? 0 : (ksp == 1 ? 22 : 43), nst = ksp == 0 ? 22 : 21;
  const u16* o = ows + (size_t)bb * SQ * HD + (size_t)s0 * SQ * 64;
  const u16* wo = (blk ? woB : woA) + (size_t)s0 * 64 * DM + nt * 128;
  float* p = part + ((size_t)ksp * 16 + bb) * SQ * DM + nt * 128;
  const int tid = threadIdx.x, wid = __builtin_amdgcn_readfirstlane(tid >> 6);
  const int rot = (3 * (r / 5) + 4 * nt) % 21;
  if (wid < 2) oproj_wave<5, 5>(o, wo, p, lds, tid, wid, nst, rot);
  else if (wid < 4) oproj_wave<5, 4>(o, wo, p, lds, tid, wid, nst, rot);
  else oproj_wave<4, 4>(o, wo, p, lds, tid, wid, nst, rot);
  asm volatile("s_waitcnt vmcnt(0)" ::: "memory");
  __syncthreads();
  if (tid == 0) {
    unsigned* c = cnt + (bb * 5 + nt) * 16;
    __hip_atomic_fetch_add(c, 1u, __ATOMIC_RELAXED, __HIP_MEMORY_SCOPE_AGENT);
    for (int spin = 0; spin < (1 << 22); ++spin) {
      if (__hip_atomic_load(c, __ATOMIC_RELAXED, __HIP_MEMORY_SCOPE_AGENT) >= (unsigned)OP_SPLIT) break;
      __builtin_amdgcn_s_sleep(2);
    }
    __builtin_amdgcn_fence(__ATOMIC_ACQUIRE, "agent");
    asm volatile("s_waitcnt vmcnt(0)" ::: "memory");
  }
  __syncthreads();
  {
    const float* p0 = part + (size_t)bb * SQ * DM + nt * 128;
#pragma unroll
    for (int it = 0; it < 3; ++it) {
      const int u = it * 512 + tid, row = ksp * 48 + (u >> 5), c4 = (u & 31) * 4;
      f32x4 sum = *(const f32x4*)(p0 + (size_t)row * DM + c4);
#pragma unroll
      for (int k = 1; k < OP_SPLIT; ++k) sum += *(const f32x4*)(p0 + (size_t)k * 16 * SQ * DM + (size_t)row * DM + c4);
      u32x2 v = {cvtpk(sum[0], sum[1]), cvtpk(sum[2], sum[3])};
      *(u32x2*)(out + ((size_t)((bb & 7) * SQ + row) * 2 + blk) * DM + nt * 128 + c4) = v;
    }
  }
}

constexpr int SLOT = 16384, NSLOT = 5, NTILE = SK / 32;
constexpr float THR = 6.0f;

template <int NB, bool X5, int NP, int P0>
__device__ __forceinline__ void attn_wave(const u16* Xg, const u16* Wg, const u16* Kg, const u16* Vg, u16* __restrict__ Og,
                                          char* lds, int tid, int wid, int qb0) {
  const int lane = tid & 63, g = lane >> 4, i16 = lane & 15;
  lds_cptr l3 = (lds_cptr)lds;
  const unsigned lds0 = (unsigned)(uintptr_t)lds;
  const unsigned kvk = g * 256 + ((i16 ^ g) << 4), kvv = g * 256 + ((i16 ^ (g << 1)) << 4);
#define ISSUE1(t, slot, i) do { const int p_ = P0 + (i); \
    if (p_ < 8) glds16((const char*)Kg + (size_t)(t) * 8192 + p_ * 1024, kvk ^ ((p_ & 3) << 6), (unsigned)__builtin_amdgcn_readfirstlane(lds0 + (slot) * SLOT + p_ * 1024)); \
    else glds16((const char*)Vg + (size_t)(t) * 8192 + (p_ - 8) * 1024, kvv ^ ((p_ & 1) << 7), (unsigned)__builtin_amdgcn_readfirstlane(lds0 + (slot) * SLOT + p_ * 1024)); } while (0)
#define ISSUE(t, slot) do { if (NP > 0) ISSUE1(t, slot, 0); if (NP > 1) ISSUE1(t, slot, 1); if (NP > 2) ISSUE1(t, slot, 2); if (NP > 3) ISSUE1(t, slot, 3); if (NP > 4) ISSUE1(t, slot, 4); if (NP > 5) ISSUE1(t, slot, 5); } while (0)
  bf16x8 qf[NB][4];
  {
    constexpr int NST = DM / 32, XB = 16384, XSZ = 18432, WB = 53248;
    const int xrow = wid * 8 + (lane >> 3);
    const unsigned xoff = xrow * (DM * 2) + (((lane & 7) ^ (xrow & 7)) << 4);
    unsigned woff[2];
#pragma unroll
    for (int it = 0; it < 2; ++it) {
      const int row = wid * 8 + it * 4 + g;
      woff[it] = row * (HD * 2) + ((i16 ^ (((row & 3) | ((row >> 1) & 4)) << 1)) << 4);
    }
    constexpr bool x5 = X5;
    const int qrot = 0;
#define RS(s_) (((s_) + qrot >= NST / 2) ? (s_) + qrot - NST / 2 : (s_) + qrot)
#define WISSUE1(c, it) glds16c((const char*)Wg + (size_t)(RS((c) >> 1) * 2 + ((c) & 1)) * (32 * HD * 2), woff[it], (unsigned)__builtin_amdgcn_readfirstlane(lds0 + WB + ((c) % 3) * 8192 + wid * 2048 + (it) * 1024))
#define WISSUE(c) do { WISSUE1(c, 0); WISSUE1(c, 1); } while (0)
#define XISSUE1(s_, it) do { const char* xs_ = (const char*)Xg + RS(s_) * 128; \
      if ((it) < 4) glds16c(xs_ + (it) * 32 * DM * 2, xoff, (unsigned)__builtin_amdgcn_readfirstlane(lds0 + XB + ((s_) & 1) * XSZ + (wid + 4 * (it)) * 1024)); \
      else if (x5) glds16c(xs_ + (128 - 16) * DM * 2, xoff, (unsigned)__builtin_amdgcn_readfirstlane(lds0 + XB + ((s_) & 1) * XSZ + (wid + 14) * 1024)); } while (0)
#define XISSUE(s_) do { XISSUE1(s_, 0); XISSUE1(s_, 1); XISSUE1(s_, 2); XISSUE1(s_, 3); XISSUE1(s_, 4); } while (0)
#define VMW(n4, n5, tail) do { if (x5) asm volatile("s_waitcnt vmcnt(%0)" tail :: "i"(n5) : "memory"); else asm volatile("s_waitcnt vmcnt(%0)" tail :: "i"(n4) : "memory"); } while (0)
    f32x4 acc[NB][8];
#pragma unroll
    for (int qb = 0; qb < NB; ++qb)
#pragma unroll
      for (int db = 0; db < 8; ++db) acc[qb][db] = f32x4{0.f, 0.f, 0.f, 0.f};
    XISSUE(0); WISSUE(0); WISSUE(1); XISSUE(1); WISSUE(2);
    ISSUE(0, 0);
    const int wr = 8 * g + (i16 >> 2);
    const int wrd = WB + wr * 256 + ((((wr & 3) | ((wr >> 1) & 4))) << 5) + (lane & 3) * 8;
    const int xrd = XB + (qb0 * 16 + i16) * 128 + ((g ^ (i16 & 7)) << 4);
    bf16x8 wf[8], xa[NB], xb_[NB];
#define WRD(db, c_) do { const s16x4 lo = vtr(l3 + ((c_) % 3) * 8192 + (wrd ^ ((db) << 5))); const s16x4 hi = vtr(l3 + ((c_) % 3) * 8192 + (wrd ^ ((db) << 5)) + 1024); \
        wf[db] = (bf16x8){lo[0], lo[1], lo[2], lo[3], hi[0], hi[1], hi[2], hi[3]}; } while (0)
#define XRD(dst, c_) do { _Pragma("unroll") for (int qb = 0; qb < NB; ++qb) dst[qb] = ldsb128(l3 + (((c_) >> 1) & 1) * XSZ + (xrd ^ (((c_) & 1) << 6)) + qb * 2048); } while (0)
    asm volatile("s_waitcnt vmcnt(%0)\n\ts_barrier" :: "i"((X5 ? 5 : 4) + 2 + NP) : "memory");
#pragma unroll
    for (int db = 0; db < 8; ++db) WRD(db, 0);
    XRD(xa, 0);
#pragma unroll
    for (int c = 0; c < NST; ++c) {
      if (c == 0) asm volatile("s_waitcnt lgkmcnt(0)\n\ts_barrier" ::: "memory");
      else if (c == 1) asm volatile("s_waitcnt vmcnt(%0) lgkmcnt(0)\n\ts_barrier" :: "i"(2 + NP) : "memory");
      else if (c + 1 < NST) {
        if (c & 1) asm volatile("s_waitcnt vmcnt(%0) lgkmcnt(0)\n\ts_barrier" :: "i"((c + 2 < NST) ? 2 : 0) : "memory");
        else VMW(((c + 2 < NST) ? 2 : 0) + ((((c - 1) >> 1) + 2 < NST / 2) ? 4 : 0), ((c + 2 < NST) ? 2 : 0) + ((((c - 1) >> 1) + 2 < NST / 2) ? 5 : 0), " lgkmcnt(0)\n\ts_barrier");
      }
      if (c + 1 < NST) { if (c & 1) XRD(xa, c + 1); else XRD(xb_, c + 1); }
      SBAR();
#pragma unroll
      for (int db = 0; db < 8; ++db) {
#pragma unroll
        for (int qb = 0; qb < NB; ++qb) acc[qb][db] = MFMA16(wf[db], (c & 1) ? xb_[qb] : xa[qb], acc[qb][db]);
        if (c + 1 < NST) WRD(db, c + 1);
        if (db < 2 && c + 3 < NST) WISSUE1(c + 3, db);
        if (db >= 2 && db < 7 && (c & 1) && (c >> 1) + 2 < NST / 2) XISSUE1((c >> 1) + 2, db - 2);
        SBAR();
      }
    }
#undef WRD
#undef XRD
#undef WISSUE
#undef XISSUE
#undef WISSUE1
#undef XISSUE1
    asm volatile("s_waitcnt lgkmcnt(0)\n\ts_barrier" ::: "memory");
    ISSUE(1, 1); ISSUE(2, 2); ISSUE(3, 3);
    char* scr = lds + 4 * SLOT + wid * 4096;
#pragma unroll
    for (int qb = 0; qb < NB; ++qb) {
#pragma unroll
      for (int db = 0; db < 8; ++db) {
        u32x2 v = {cvtpk(acc[qb][db][0] * QSCALE, acc[qb][db][1] * QSCALE), cvtpk(acc[qb][db][2] * QSCALE, acc[qb][db][3] * QSCALE)};
        *(u32x2*)(scr + i16 * 256 + (((db * 2 + (g >> 1)) ^ i16) << 4) + (g & 1) * 8) = v;
      }
      asm volatile("s_waitcnt lgkmcnt(0)" ::: "memory");
#pragma unroll
      for (int ks = 0; ks < 4; ++ks) qf[qb][ks] = *(const bf16x8*)(scr + i16 * 256 + (((ks * 4 + g) ^ i16) << 4));
      asm volatile("s_waitcnt lgkmcnt(0)" ::: "memory");
#pragma unroll
      for (int ks = 0; ks < 4; ++ks) PIN(qf[qb][ks]);
    }
  }

  f32x4 o[NB][8], ls[NB], negm[NB];
#pragma unroll
  for (int qb = 0; qb < NB; ++qb) {
#pragma unroll
    for (int db = 0; db < 8; ++db) o[qb][db] = f32x4{0.f, 0.f, 0.f, 0.f};
    ls[qb] = f32x4{0.f, 0.f, 0.f, 0.f};
    negm[qb] = f32x4{0.f, 0.f, 0.f, 0.f};
  }
  const bf16x8 ones = {0x3F80, 0x3F80, 0x3F80, 0x3F80, 0x3F80, 0x3F80, 0x3F80, 0x3F80};
  const int kbase = i16 * 256 + ((g ^ i16) << 4);
  const int vrow = 4 * g + (i16 >> 2);
  const int vbase = 8192 + vrow * 256 + ((vrow & 7) << 5) + (lane & 3) * 8;
  f32x4 s[2][NB];
  bf16x8 pf[NB], pn[NB];
  bf16x8 kf[8], vf[8];

#define KRD(i, so_) kf[i] = ldsb128(l3 + (so_) + (kbase ^ (((i) >> 1) << 6)) + ((i) & 1) * 4096)
#define VRD(db, so_) do { const s16x4 lo = vtr(l3 + (so_) + (vbase ^ ((db) << 5))); const s16x4 hi = vtr(l3 + (so_) + (vbase ^ ((db) << 5)) + 4096); \
      vf[db] = (bf16x8){lo[0], lo[1], lo[2], lo[3], hi[0], hi[1], hi[2], hi[3]}; } while (0)
#define QKI(i) do { _Pragma("unroll") for (int qb = 0; qb < NB; ++qb) s[(i) & 1][qb] = MFMA16(kf[i], qf[qb][(i) >> 1], ((i) >> 1) == 0 ? negm[qb] : s[(i) & 1][qb]); } while (0)
#define ROWMAX(qb) fmx(fmx(fmx(fmx(s[0][qb][0], s[0][qb][1]), s[0][qb][2]), fmx(fmx(s[0][qb][3], s[1][qb][0]), s[1][qb][1])), fmx(s[1][qb][2], s[1][qb][3]))
#define XLANE_MAX(bm) do { \
        { auto r = __builtin_amdgcn_permlane16_swap(__float_as_uint(bm), __float_as_uint(bm), false, false); bm = fmx(__uint_as_float(r[0]), __uint_as_float(r[1])); } \
        { auto r = __builtin_amdgcn_permlane32_swap(__float_as_uint(bm), __float_as_uint(bm), false, false); bm = fmx(__uint_as_float(r[0]), __uint_as_float(r[1])); } } while (0)
#define EXPP(pi) do { unsigned w_ = cvtpk(__builtin_amdgcn_exp2f(s[((pi) >> 1) & 1][(pi) >> 2][((pi) & 1) * 2]), __builtin_amdgcn_exp2f(s[((pi) >> 1) & 1][(pi) >> 2][((pi) & 1) * 2 + 1])); \
      PIN(w_); pw[(pi) >> 2][(pi) & 3] = w_; } while (0)
#define PVI(db) do { _Pragma("unroll") for (int qb = 0; qb < NB; ++qb) o[qb][db] = MFMA16(vf[db], pf[qb], o[qb][db]); } while (0)
#define LSUM() do { _Pragma("unroll") for (int qb = 0; qb < NB; ++qb) ls[qb] = MFMA16(ones, pf[qb], ls[qb]); } while (0)

  asm volatile("s_waitcnt vmcnt(%0) lgkmcnt(0)\n\ts_barrier" :: "i"(2 * NP) : "memory");
  u32x4 pw[NB];
#pragma unroll
  for (int i = 0; i < 8; ++i) KRD(i, 0);
#pragma unroll
  for (int i = 0; i < 8; ++i) QKI(i);
#pragma unroll
  for (int qb = 0; qb < NB; ++qb) {
    float bm = ROWMAX(qb); XLANE_MAX(bm);
    negm[qb] -= bm; s[0][qb] -= bm; s[1][qb] -= bm;
  }
#pragma unroll
  for (int pi = 0; pi < 4 * NB; ++pi) EXPP(pi);
#pragma unroll
  for (int qb = 0; qb < NB; ++qb) pf[qb] = __builtin_bit_cast(bf16x8, pw[qb]);
  KRD(0, SLOT); KRD(1, SLOT);
  int sprev = 0, scur = 1;
  for (int t = 1; t < NTILE; ++t) {
    const int snext = (scur == NSLOT - 1) ? 0 : scur + 1;
    const int so = scur * SLOT, sp = sprev * SLOT;
    SBAR();
#pragma unroll
    for (int i = 0; i < 8; ++i) {
      if (i + 2 < 8) KRD(i + 2, so);
      else VRD(i - 6, sp);
      QKI(i);
      SBAR();
    }
    float mx[NB]; bool trig = false;
#pragma unroll
    for (int qb = 0; qb < NB; ++qb) { mx[qb] = ROWMAX(qb); trig |= (mx[qb] > THR); }
    float al[NB]; bool resc = false;
    if (__builtin_expect(__any(trig), 0)) {
      resc = true;
#pragma unroll
      for (int qb = 0; qb < NB; ++qb) {
        float bm = mx[qb]; XLANE_MAX(bm);
        const float d = fmx(bm, 0.f);
        negm[qb] -= d; s[0][qb] -= d; s[1][qb] -= d;
        al[qb] = __builtin_amdgcn_exp2f(-d);
      }
    }
    if (t + 2 < NTILE) asm volatile("s_waitcnt vmcnt(%0)\n\ts_barrier" :: "i"(NP) : "memory");
    else if (t + 1 < NTILE) asm volatile("s_waitcnt vmcnt(0)\n\ts_barrier" ::: "memory");
    const int ns = (sprev == 0) ? (NSLOT - 1) : (sprev - 1);
    const bool more = (t + 3 < NTILE);
    SBAR();
    LSUM();
#pragma unroll
    for (int db = 0; db < 8; ++db) {
      if (db + 2 < 8) VRD(db + 2, sp);
      else if (t + 1 < NTILE) KRD(db - 6, snext * SLOT);
      PVI(db);
      if (db < NP && more) ISSUE1(t + 3, ns, (db < NP ? db : 0));
      if (NB == 3) { EXPP((3 * db) >> 1); if (db & 1) EXPP(((3 * db) >> 1) + 1); }
      else EXPP(db);
      SBAR();
    }
#pragma unroll
    for (int qb = 0; qb < NB; ++qb) pf[qb] = __builtin_bit_cast(bf16x8, pw[qb]);
    if (__builtin_expect(resc, 0)) {
#pragma unroll
      for (int qb = 0; qb < NB; ++qb) {
#pragma unroll
        for (int db = 0; db < 8; ++db) o[qb][db] *= al[qb];
        ls[qb] *= al[qb];
      }
    }
    sprev = scur; scur = snext;
  }
  LSUM();
#pragma unroll
  for (int db = 0; db < 8; ++db) { VRD(db, sprev * SLOT); PVI(db); }
#undef ISSUE
#pragma unroll
  for (int qb = 0; qb < NB; ++qb) {
    const float inv = 1.0f / ls[qb][0];
#pragma unroll
    for (int db = 0; db < 8; ++db) {
      u32x2 v = {cvtpk(o[qb][db][0] * inv, o[qb][db][1] * inv), cvtpk(o[qb][db][2] * inv, o[qb][db][3] * inv)};
      *(u32x2*)(Og + (size_t)((db >> 2) * SQ + (qb0 + qb) * 16 + i16) * 64 + (db & 3) * 16 + g * 4) = v;
    }
  }
}

__global__ __launch_bounds__(256, 2) void attn_kernel(const u16* __restrict__ xA, const u16* __restrict__ xB, const u16* __restrict__ wqA, const u16* __restrict__ wqB,
                                                      const u16* __restrict__ kA, const u16* __restrict__ vA,
                                                      const u16* __restrict__ kB, const u16* __restrict__ vB, u16* __restrict__ ows, unsigned* __restrict__ cnt) {
  extern __shared__ __attribute__((aligned(16))) char lds[];
  if (blockIdx.x < 80 && threadIdx.x == 0) cnt[blockIdx.x * 16] = 0u;
  const int item = blockIdx.x, blk = item >> 8, bh = item & 255, b = bh >> 5, h = bh & 31;
  const u16* K = (blk ? kB : kA) + (size_t)bh * SK * DH;
  const u16* V = (blk ? vB : vA) + (size_t)bh * SK * DH;
  const u16* X = (blk ? xB : xA) + (size_t)b * SQ * DM;
  const u16* W = (blk ? wqB : wqA) + h * DH;
  u16* O = ows + ((size_t)(blk * 8 + b) * SQ) * HD + (size_t)h * SQ * DH;
  const int tid = threadIdx.x, wid = __builtin_amdgcn_readfirstlane(tid >> 6);
  if (wid == 0) attn_wave<3, false, 0, 0>(X, W, K, V, O, lds, tid, wid, 0);
  else if (wid == 1) attn_wave<2, false, 6, 0>(X, W, K, V, O, lds, tid, wid, 3);
  else if (wid == 2) attn_wave<2, true, 5, 6>(X, W, K, V, O, lds, tid, wid, 5);
  else attn_wave<2, true, 5, 11>(X, W, K, V, O, lds, tid, wid, 7);
}

extern "C" void kernel_launch(void* const* d_in, const int* in_sizes, int n_in, void* d_out, int out_size, void* d_ws,
                              size_t ws_size, hipStream_t stream) {
  const u16* xA = (const u16*)d_in[0]; const u16* kA = (const u16*)d_in[1]; const u16* vA = (const u16*)d_in[2];
  const u16* wqA = (const u16*)d_in[3]; const u16* woA = (const u16*)d_in[4];
  const u16* xB = (const u16*)d_in[5]; const u16* kB = (const u16*)d_in[6]; const u16* vB = (const u16*)d_in[7];
  const u16* wqB = (const u16*)d_in[8]; const u16* woB = (const u16*)d_in[9];
  char* ws = (char*)d_ws;
  unsigned* cnt = (unsigned*)ws;
  u16* ows = (u16*)(ws + 8192);
  float* part = (float*)(ws + 8192 + (size_t)16 * SQ * HD * 2);
  (void)hipFuncSetAttribute((const void*)attn_kernel, hipFuncAttributeMaxDynamicSharedMemorySize, NSLOT * SLOT);
  (void)hipFuncSetAttribute((const void*)oproj_kernel, hipFuncAttributeMaxDynamicSharedMemorySize, OP_NSTG * OP_STAGE);
  attn_kernel<<<512, 256, NSLOT * SLOT, stream>>>(xA, xB, wqA, wqB, kA, vA, kB, vB, ows, cnt);
  oproj_kernel<<<240, 512, OP_NSTG * OP_STAGE, stream>>>(ows, woA, woB, part, cnt, (u16*)d_out);
}
```
